```python
import torch

def _softmax(x, axis=-1):
    return torch.softmax(x, dim=axis)
def _gelu(x):
    return torch.nn.functional.gelu(x, approximate="tanh")
def _silu(x):
    return torch.nn.functional.silu(x)
def _sigmoid(x):
    return torch.sigmoid(x)
def _layernorm(x, axis=-1, eps=1e-5):
    m = x.mean(dim=axis, keepdim=True)
    v = x.var(dim=axis, keepdim=True, unbiased=False)
    return (x - m) / torch.sqrt(v + eps)
def _scatter_add(tgt, idx, src, axis=0):
    return tgt.clone().index_add_(axis, idx, src)
def _segment_sum(src, seg, n):
    out = torch.zeros((n,) + src.shape[1:], dtype=src.dtype)
    return out.index_add_(0, seg, src)
def _pad(x, pw):
    flat = tuple(v for pair in reversed(pw) for v in pair)
    return torch.nn.functional.pad(x, flat)

kernel_name = 'sequential_preLN__linear_attn_block__dense_activation_mish__prompt'

def setup_inputs():

    _w = (lambda a: (lambda t: t.to(torch.bfloat16) if t.is_floating_point() else t)(torch.as_tensor(a)))
    def _A_setup():
        B, S, D = 1, 2048, 512
        return {
            "x": _w((lambda *s: torch.randn(*s, dtype=torch.bfloat16) * 0.05)(B, S, D)),
            "wqkv": _w((lambda *s: torch.randn(*s, dtype=torch.bfloat16) * 0.05)(D, 3 * D)),
            "wo": _w((lambda *s: torch.randn(*s, dtype=torch.bfloat16) * 0.05)(D, D)),
        }
    _A_in = _A_setup()

    def _B_setup():
        B, S, D, F = 1, 2048, 512, 1792
        return {
            "x": _w((lambda *s: torch.randn(*s, dtype=torch.bfloat16) * 0.05)(B, S, D)),
            "w1": _w((lambda *s: torch.randn(*s, dtype=torch.bfloat16) * 0.8)(D, F) * (D ** -0.5)),
            "b1": _w((lambda *s: torch.randn(*s, dtype=torch.bfloat16) * 0.2)(F)),
            "w2": _w((lambda *s: torch.randn(*s, dtype=torch.bfloat16) * 0.1)(F, D) * (F ** -0.5)),
        }
    _B_in = _B_setup()

    return {
        "A_x": _A_in["x"],
        "A_wqkv": _A_in["wqkv"],
        "A_wo": _A_in["wo"],
        "B_x": _B_in["x"],
        "B_w1": _B_in["w1"],
        "B_b1": _B_in["b1"],
        "B_w2": _B_in["w2"],
    }

def reference(A_x, A_wqkv, A_wo, B_x, B_w1, B_b1, B_w2):
    def _A_ref(x, wqkv, wo):
        B, S, D = x.shape
        H = 32; Dh = D // H
        qkv = (lambda x, *d: x.permute(*d))((x @ wqkv).reshape(B, S, 3, H, Dh), 2, 0, 3, 1, 4)
        q, k, v = qkv[0], qkv[1], qkv[2]
        phi = lambda t: torch.where(t > 0, t + 1, torch.exp(t)) + 1e-6
        pq, pk = phi(q), phi(k)
        kv = torch.einsum("bhsd,bhse->bhde", pk, v)
        z = torch.einsum("bhsd->bhd", pk)
        num = torch.einsum("bhqd,bhde->bhqe", pq, kv)
        den = torch.einsum("bhqd,bhd->bhq", pq, z)[..., None] + 1e-6
        o = num / den
        return (lambda x, *d: x.permute(*d))(o, 0, 2, 1, 3).reshape(B, S, D) @ wo

    def _B_ref(x, w1, b1, w2):
        h = torch.einsum("bsd,df->bsf", x, w1) + b1
        sp = torch.log1p(torch.exp(-torch.abs(h))) + torch.where(h > 0, h, torch.zeros_like(h))
        act = h * torch.tanh(sp)
        return torch.einsum("bsf,fd->bsd", act, w2)

    x = A_x
    h = x + _A_ref(_layernorm(x, axis=-1), A_wqkv, A_wo)
    h2 = h + _B_ref(_layernorm(h, axis=-1), B_w1, B_b1, B_w2)
    return h2
```

```cpp
#include <hip/hip_runtime.h>
#include <hip/hip_bf16.h>
typedef unsigned short u16;
__device__ __forceinline__ float bf2f(u16 b){ return __uint_as_float(((unsigned)b)<<16); }
__device__ __forceinline__ u16 f2bf(float f){ unsigned u=__float_as_uint(f); u += 0x7fffu + ((u>>16)&1u); return (u16)(u>>16);}
__device__ __forceinline__ float rbf(float f){ return bf2f(f2bf(f)); }
constexpr int S=2048,D=512,F=1792,H=32,DH=16;

__global__ void ln_kernel(const u16* x, u16* y){
  int row=blockIdx.x; int t=threadIdx.x;
  float v[8]; float s=0;
  for(int i=0;i<8;i++){ v[i]=bf2f(x[row*D+t*8+i]); s+=v[i]; }
  for(int o=32;o>0;o>>=1) s+=__shfl_xor(s,o);
  float m=s/D; float q=0;
  for(int i=0;i<8;i++){ float d=v[i]-m; q+=d*d; }
  for(int o=32;o>0;o>>=1) q+=__shfl_xor(q,o);
  float var=q/D; float mb=rbf(m); float sd=rbf(sqrtf(rbf(rbf(var)+1e-5f)));
  for(int i=0;i<8;i++){ y[row*D+t*8+i]=f2bf(rbf(v[i]-mb)/sd); }
}
template<int MODE>
__global__ void gemm_naive(const u16* A,const u16* B,u16* C,const u16* aux,int M,int N,int K){
  __shared__ float sA[16][17], sB[16][17];
  int tx=threadIdx.x, ty=threadIdx.y; int r=blockIdx.y*16+ty, c=blockIdx.x*16+tx;
  float acc=0;
  for(int k0=0;k0<K;k0+=16){
    sA[ty][tx]=bf2f(A[r*K+k0+tx]); sB[ty][tx]=bf2f(B[(k0+ty)*N+c]);
    __syncthreads();
    for(int k=0;k<16;k++) acc+=sA[ty][k]*sB[k][tx];
    __syncthreads();
  }
  if(MODE==0) C[r*N+c]=f2bf(acc);
  if(MODE==1) C[r*N+c]=f2bf(bf2f(aux[r*N+c])+rbf(acc));
  if(MODE==2){ float h=rbf(rbf(acc)+bf2f(aux[c])); float sp=log1pf(expf(-fabsf(h)))+(h>0?h:0.f); C[r*N+c]=f2bf(h*tanhf(sp)); }
}
__device__ __forceinline__ float phi(float t){ return (t>0? t+1.f : expf(t))+1e-6f; }
__global__ void kv_kernel(const u16* qkv, float* kv){
  int h=blockIdx.x, t=threadIdx.x; int d=t>>4, e=t&15; float acc=0;
  if(t<256){ for(int s=0;s<S;s++) acc+=rbf(phi(bf2f(qkv[s*1536+512+h*16+d])))*bf2f(qkv[s*1536+1024+h*16+e]); kv[h*272+t]=rbf(acc);}
  else if(t<272){ d=t-256; for(int s=0;s<S;s++) acc+=rbf(phi(bf2f(qkv[s*1536+512+h*16+d]))); kv[h*272+t]=rbf(acc);}
}
__global__ void o_kernel(const u16* qkv,const float* kv,u16* o){
  int s=blockIdx.x, c=threadIdx.x; int h=c>>4,e=c&15; float num=0,den=0;
  for(int d=0;d<16;d++){ float p=rbf(phi(bf2f(qkv[s*1536+h*16+d]))); num+=p*kv[h*272+d*16+e]; den+=p*kv[h*272+256+d]; }
  o[s*D+c]=f2bf(rbf(num)/rbf(rbf(den)+1e-6f));
}
extern "C" void kernel_launch(void* const* d_in, const int* in_sizes, int n_in, void* d_out, int out_size, void* d_ws, size_t ws_size, hipStream_t stream){
  const u16* x=(const u16*)d_in[0]; const u16* wqkv=(const u16*)d_in[1]; const u16* wo=(const u16*)d_in[2];
  const u16* w1=(const u16*)d_in[4]; const u16* b1=(const u16*)d_in[5]; const u16* w2=(const u16*)d_in[6];
  char* ws=(char*)d_ws; u16* xn=(u16*)ws; u16* qkv=(u16*)(ws+(4<<20)); float* kv=(float*)(ws+(12<<20)); u16* o=(u16*)(ws+(13<<20));
  u16* h=(u16*)(ws+(16<<20)); u16* hn=(u16*)(ws+(20<<20)); u16* act=(u16*)(ws+(24<<20));
  ln_kernel<<<S,64,0,stream>>>(x,xn);
  gemm_naive<0><<<dim3(1536/16,S/16),dim3(16,16),0,stream>>>(xn,wqkv,qkv,nullptr,S,1536,D);
  kv_kernel<<<H,320,0,stream>>>(qkv,kv);
  o_kernel<<<S,512,0,stream>>>(qkv,kv,o);
  gemm_naive<1><<<dim3(D/16,S/16),dim3(16,16),0,stream>>>(o,wo,h,x,S,D,D);
  ln_kernel<<<S,64,0,stream>>>(h,hn);
  gemm_naive<2><<<dim3(F/16,S/16),dim3(16,16),0,stream>>>(hn,w1,act,b1,S,F,D);
  gemm_naive<1><<<dim3(D/16,S/16),dim3(16,16),0,stream>>>(act,w2,(u16*)d_out,h,S,D,F);
}
```

```cpp
#include <hip/hip_runtime.h>
#include <hip/hip_bf16.h>

typedef unsigned short u16;
typedef unsigned v4u __attribute__((ext_vector_type(4)));
typedef unsigned v2u __attribute__((ext_vector_type(2)));
typedef float f4 __attribute__((ext_vector_type(4)));
typedef float f2 __attribute__((ext_vector_type(2)));
typedef short s4 __attribute__((ext_vector_type(4)));
typedef __bf16 b8 __attribute__((ext_vector_type(8)));
typedef __bf16 b2 __attribute__((ext_vector_type(2)));
#define GAS __attribute__((address_space(1)))
#define LAS __attribute__((address_space(3)))
#define DI __device__ __forceinline__
#define RLX_AGENT __ATOMIC_RELAXED, __HIP_MEMORY_SCOPE_AGENT

#define MAPMODE 0

constexpr int WS_EPOCH = 0;
constexpr int WS_FLAG = 4096;
constexpr int WS_KV = 65536;
constexpr int KV_REC = 2304;
constexpr int WS_O = 1 << 20;
constexpr int WS_H = 3 << 20;
constexpr int WS_A = 5 << 20;
constexpr int WS_BYTES = 12 << 20;
#define STAMP(n)
#define STAMP_DECL
#define STAMP_FLUSH
#define COUNT(n)

constexpr int L_WK = 0, L_WV = 32768, L_WQ = 65536, L_WO = 98304, L_W1S = 131072, L_SCR = 147456;
constexpr int L_KVRED = L_W1S;
constexpr int L_CONS = L_SCR;
constexpr int L_ZP = L_SCR + 8192;
constexpr int L_ZC = L_ZP + 1024;
constexpr int L_CQ = L_ZC + 512;
constexpr int L_C1 = L_CQ + 512;
constexpr int L_B1 = L_C1 + 512;
constexpr int LDS_BYTES = 163840;

constexpr unsigned POISON = 0xFFFFFFFFu;
constexpr int SC1 = 16;
constexpr float LOG2E = 1.4426950408889634f;

DI float asf(unsigned u) { return __builtin_bit_cast(float, u); }
DI unsigned asu(float f) { return __builtin_bit_cast(unsigned, f); }
DI f4 mfma32(v4u a, v4u b, f4 c) { return __builtin_amdgcn_mfma_f32_16x16x32_bf16(__builtin_bit_cast(b8, a), __builtin_bit_cast(b8, b), c, 0, 0, 0); }
DI f4 mfma16(v2u a, v2u b, f4 c) { return __builtin_amdgcn_mfma_f32_16x16x16bf16_1k(__builtin_bit_cast(s4, a), __builtin_bit_cast(s4, b), c, 0, 0, 0); }
DI unsigned pk(float lo, float hi) { f2 v = {lo, hi}; return __builtin_bit_cast(unsigned, __builtin_convertvector(v, b2)); }
DI float bflo(unsigned u) { return __builtin_bit_cast(float, u << 16); }
DI float bfhi(unsigned u) { return __builtin_bit_cast(float, u & 0xffff0000u); }
DI unsigned nopoison(unsigned u) { return u < 0xFFFFFFFEu ? u : 0xFFFFFFFEu; }
DI v2u trd(LAS char* p) { return __builtin_bit_cast(v2u, __builtin_amdgcn_ds_read_tr16_b64_v4i16((LAS s4*)p)); }
DI v4u trfrag(LAS char* p, int second) { v2u a = trd(p), b = trd(p + second); return v4u{a.x, a.y, b.x, b.y}; }
DI float dot2(unsigned a, unsigned b, float c) { return __builtin_amdgcn_fdot2_f32_bf16(__builtin_bit_cast(b2, a), __builtin_bit_cast(b2, b), c, false); }
DI float rowsum4(float v) {
  unsigned u = __builtin_bit_cast(unsigned, v);
  auto r = __builtin_amdgcn_permlane32_swap(u, u, false, false);
  float s = asf(r[0]) + asf(r[1]);
  u = __builtin_bit_cast(unsigned, s);
  auto t = __builtin_amdgcn_permlane16_swap(u, u, false, false);
  return asf(t[0]) + asf(t[1]);
}
DI v4u mksrd(const void* p, unsigned bytes) {
  unsigned long long a = (unsigned long long)p;
  return v4u{(unsigned)a, (unsigned)(a >> 32) & 0xffffu, bytes, 0x00020000u};
}
DI void dma16(v4u srd, unsigned voff, unsigned soff, unsigned lds) {
  asm volatile("s_mov_b32 m0, %3\n\ts_nop 0\n\tbuffer_load_dwordx4 %0, %1, %2 offen lds" ::"v"(voff), "s"(srd), "s"(soff), "s"(lds) : "memory");
}
DI void st16(v4u v, __amdgpu_buffer_rsrc_t r, int voff, int soff) {
  __builtin_amdgcn_raw_buffer_store_b128(v, r, voff, soff, SC1);
  asm volatile("s_nop 1" ::"v"(v) : "memory");
}
#define BAR() do { asm volatile("s_waitcnt lgkmcnt(0)" ::: "memory"); __builtin_amdgcn_s_barrier(); asm volatile("" ::: "memory"); } while (0)
#define VMWAIT(n) asm volatile("s_waitcnt vmcnt(%0)" ::"i"(n) : "memory")

#define CND(d, s0, s1, qp) "v_cndmask_b32_dpp " d ", " s0 ", " s1 ", vcc quad_perm:" qp " row_mask:0xf bank_mask:0xf\n\t"
DI void qswap1(v4u& a, v4u& b) {
  unsigned n0, n1, n2, n3, m0, m1, m2, m3;
  asm volatile("s_nop 1\n\ts_mov_b64 vcc, %16\n\t" CND("%0", "%12", "%8", "[1,0,3,2]") CND("%1", "%13", "%9", "[1,0,3,2]") CND("%2", "%14", "%10", "[1,0,3,2]") CND("%3", "%15", "%11", "[1,0,3,2]")
               "s_not_b64 vcc, vcc\n\t" CND("%4", "%8", "%12", "[1,0,3,2]") CND("%5", "%9", "%13", "[1,0,3,2]") CND("%6", "%10", "%14", "[1,0,3,2]") CND("%7", "%11", "%15", "[1,0,3,2]")
               : "=&v"(n0), "=&v"(n1), "=&v"(n2), "=&v"(n3), "=&v"(m0), "=&v"(m1), "=&v"(m2), "=&v"(m3)
               : "v"(a.x), "v"(a.y), "v"(a.z), "v"(a.w), "v"(b.x), "v"(b.y), "v"(b.z), "v"(b.w), "s"(0x5555555555555555ull) : "vcc", "scc");
  a = v4u{n0, n1, n2, n3}; b = v4u{m0, m1, m2, m3};
}
DI void qswap2(v4u& a, v4u& b) {
  unsigned n0, n1, n2, n3, m0, m1, m2, m3;
  asm volatile("s_nop 1\n\ts_mov_b64 vcc, %16\n\t" CND("%0", "%12", "%8", "[2,3,0,1]") CND("%1", "%13", "%9", "[2,3,0,1]") CND("%2", "%14", "%10", "[2,3,0,1]") CND("%3", "%15", "%11", "[2,3,0,1]")
               "s_not_b64 vcc, vcc\n\t" CND("%4", "%8", "%12", "[2,3,0,1]") CND("%5", "%9", "%13", "[2,3,0,1]") CND("%6", "%10", "%14", "[2,3,0,1]") CND("%7", "%11", "%15", "[2,3,0,1]") "s_nop 1"
               : "=&v"(n0), "=&v"(n1), "=&v"(n2), "=&v"(n3), "=&v"(m0), "=&v"(m1), "=&v"(m2), "=&v"(m3)
               : "v"(a.x), "v"(a.y), "v"(a.z), "v"(a.w), "v"(b.x), "v"(b.y), "v"(b.z), "v"(b.w), "s"(0x3333333333333333ull) : "vcc", "scc");
  a = v4u{n0, n1, n2, n3}; b = v4u{m0, m1, m2, m3};
}
DI unsigned umax3(unsigned a, unsigned b, unsigned c) { return max(max(a, b), c); }
DI unsigned fragmax(unsigned m, v4u f) { return umax3(umax3(m, f.x, f.y), f.z, f.w); }

DI float colsum_tile(LAS char* p, int step, int second) {
  const v4u ones = {0x3F803F80u, 0x3F803F80u, 0x3F803F80u, 0x3F803F80u};
  f4 c = {0.f, 0.f, 0.f, 0.f};
#pragma unroll 4
  for (int s = 0; s < 16; ++s) c = mfma32(ones, trfrag(p + s * step, second), c);
  return c[0];
}
DI float phi(float t) { return fmaxf(t, 0.f) + __builtin_amdgcn_exp2f(fminf(t, 0.f) * LOG2E) + 1e-6f; }
DI float mish(float t) {
  float n = __builtin_amdgcn_exp2f(fminf(t, 30.f) * LOG2E);
  float u = n * (n + 2.f);
  return t * u * __builtin_amdgcn_rcpf(u + 2.f);
}

__global__ void __launch_bounds__(512, 2)
fused_block(const u16* __restrict__ x, const u16* __restrict__ wqkv, const u16* __restrict__ wo, const u16* __restrict__ w1,
            const u16* __restrict__ b1, const u16* __restrict__ w2, u16* __restrict__ out, char* ws) {
  extern __shared__ __attribute__((aligned(16))) char smem_[];
  LAS char* L = (LAS char*)smem_;
  const int tid = threadIdx.x, lane = tid & 63, w = __builtin_amdgcn_readfirstlane(tid >> 6);
  const int g = lane >> 4, i = lane & 15, q = (lane >> 2) & 3, pp = lane & 3;
  const int bid = blockIdx.x, xcd = bid & 7, rk = bid >> 3;
  const int p = 2 * xcd + (rk >> 4), j = rk & 15;
  STAMP_DECL
  STAMP(0);
  const v4u dQKV = mksrd(wqkv, 512 * 1536 * 2), dWO = mksrd(wo, 512 * 512 * 2), dW1 = mksrd(w1, 512 * 1792 * 2), dX = mksrd(x, 2048 * 512 * 2);
  const __amdgpu_buffer_rsrc_t rWS = __builtin_amdgcn_make_buffer_rsrc(ws, 0, WS_BYTES, 0x00020000);
  const __amdgpu_buffer_rsrc_t rW2 = __builtin_amdgcn_make_buffer_rsrc((void*)w2, 0, 1792 * 512 * 2, 0x00020000);
  const __amdgpu_buffer_rsrc_t rX = __builtin_amdgcn_make_buffer_rsrc((void*)x, 0, 2048 * 512 * 2, 0x00020000);
  const unsigned lds0 = (unsigned)(size_t)L;

  {
    const unsigned v0 = (lane >> 2) * 3072 + pp * 16, v1 = (lane >> 2) * 3072 + (pp ^ 2) * 16;
    const int m = w >> 2, blk0 = (w & 3) * 8;
#pragma unroll
    for (int n = 0; n < 8; ++n)
      dma16(dQKV, ((n >> 1) & 1) ? v1 : v0, (blk0 + n) * (16 * 3072) + (1 + m) * 1024 + j * 64, lds0 + m * 32768 + (blk0 + n) * 1024);
  }
  v4u xf[16];
  {
    const unsigned vo = (q * 4) * 1024 + g * 64 + pp * 16, so = (p * 128 + w * 16) * 1024;
#pragma unroll
    for (int u = 0; u < 4; ++u)
#pragma unroll
      for (int ii = 0; ii < 4; ++ii)
        asm volatile("buffer_load_dwordx4 %0, %1, %2, %3 offen offset:%4" : "=v"(xf[u * 4 + ii]) : "v"(vo), "s"(dX), "s"(so), "i"(ii * 1024 + u * 256) : "memory");
  }
  {
    const unsigned v0 = (lane >> 2) * 3072 + pp * 16, v1 = (lane >> 2) * 3072 + (pp ^ 2) * 16;
#pragma unroll
    for (int n = 0; n < 4; ++n) dma16(dQKV, ((n >> 1) & 1) ? v1 : v0, (4 * w + n) * (16 * 3072) + j * 64, lds0 + L_WQ + (4 * w + n) * 1024);
  }
  STAMP(1);
  LAS char* const tX0 = L + (32 * g + q) * 64 + pp * 8 + ((g & 1) * 32);
  LAS char* const tX1 = L + (32 * g + q) * 64 + pp * 8 + (((g & 1) ^ 1) * 32);
  LAS char* const tC0 = L + (4 * g + q) * 64 + pp * 8 + ((g & 1) * 32);
  LAS char* const tC1 = L + (4 * g + q) * 64 + pp * 8 + (((g & 1) ^ 1) * 32);

  f4 ak0 = {0, 0, 0, 0}, ak1 = ak0, av0 = ak0, av1 = ak0;
  float s0 = 0.f, s1 = 0.f, ss0 = 0.f, ss1 = 0.f;
  unsigned epoch = 0;
  v2u xr0, xr1;
#pragma unroll
  for (int u = 0; u < 4; ++u) {
    if (u == 0) asm volatile("s_waitcnt vmcnt(%4)" : "+v"(xf[0]), "+v"(xf[1]), "+v"(xf[2]), "+v"(xf[3]) : "i"(12 + 4) : "memory");
    if (u == 1) asm volatile("s_waitcnt vmcnt(%4)" : "+v"(xf[4]), "+v"(xf[5]), "+v"(xf[6]), "+v"(xf[7]) : "i"(8 + 4 + 7) : "memory");
    if (u == 2) asm volatile("s_waitcnt vmcnt(%4)" : "+v"(xf[8]), "+v"(xf[9]), "+v"(xf[10]), "+v"(xf[11]) : "i"(4 + 4 + 7 + 6) : "memory");
    if (u == 3) asm volatile("s_waitcnt vmcnt(%4)" : "+v"(xf[12]), "+v"(xf[13]), "+v"(xf[14]), "+v"(xf[15]) : "i"(4 + 7 + 6) : "memory");
    if (u == 0) {
      __builtin_amdgcn_s_barrier();
      asm volatile("" ::: "memory");
      if (w < 4) {
        LAS char* base = ((w & 1) ? tX1 : tX0) + (w >> 1) * 32768;
        const v4u ones = {0x3F803F80u, 0x3F803F80u, 0x3F803F80u, 0x3F803F80u};
        f4 c = {0.f, 0.f, 0.f, 0.f};
#pragma unroll 4
        for (int s = 0; s < 16; ++s) c = mfma32(ones, trfrag(base + ((s >> 2) * 128 + (s & 3) * 8) * 64, 256), c);
        if (g == 0) *(LAS float*)(L + L_CQ + (w * 16 + i) * 4) = c[0];
      }
    }
    qswap1(xf[4 * u + 0], xf[4 * u + 1]);
    qswap1(xf[4 * u + 2], xf[4 * u + 3]);
    qswap2(xf[4 * u + 0], xf[4 * u + 2]);
    qswap2(xf[4 * u + 1], xf[4 * u + 3]);
#pragma unroll
    for (int ii = 0; ii < 4; ++ii) {
      const v4u a = xf[4 * u + ii];
      const int off = (128 * u + 8 * ii) * 64;
      ak0 = mfma32(a, trfrag(tX0 + L_WK + off, 256), ak0);
      ak1 = mfma32(a, trfrag(tX1 + L_WK + off, 256), ak1);
      av0 = mfma32(a, trfrag(tX0 + L_WV + off, 256), av0);
      av1 = mfma32(a, trfrag(tX1 + L_WV + off, 256), av1);
      s0 = dot2(a.x, 0x3F803F80u, s0); s1 = dot2(a.y, 0x3F803F80u, s1); s0 = dot2(a.z, 0x3F803F80u, s0); s1 = dot2(a.w, 0x3F803F80u, s1);
      ss0 = dot2(a.x, a.x, ss0); ss1 = dot2(a.y, a.y, ss1); ss0 = dot2(a.z, a.z, ss0); ss1 = dot2(a.w, a.w, ss1);
    }
    if (u == 0) {
      asm volatile("" ::: "memory");
      epoch = __builtin_amdgcn_raw_buffer_load_b32(rWS, WS_EPOCH, 0, SC1);
      const int ro = (p * 128 + w * 16 + i) * 1024 + j * 64 + g * 8;
      xr0 = __builtin_amdgcn_raw_buffer_load_b64(rX, ro, 0, 0);
      xr1 = __builtin_amdgcn_raw_buffer_load_b64(rX, ro + 32, 0, 0);
      const unsigned vc = (lane >> 2) * 1024 + (pp ^ ((g & 1) << 1)) * 16;
#pragma unroll
      for (int n = 0; n < 4; ++n) dma16(dWO, vc, (4 * w + n) * (16 * 1024) + j * 64, lds0 + L_WO + (4 * w + n) * 1024);
    }
    if (u == 1) {
      asm volatile("" ::: "memory");
      const v4u pz = {POISON, POISON, POISON, POISON};
      const int so = ((p * 8 + w) * 16 + j) * 1024;
      st16(pz, rWS, lane * 16, WS_O + so);
      st16(pz, rWS, lane * 16, WS_H + so);
      const int ao = WS_A + ((p * 8 + w) * 56 + ((7 * j + (j & 1)) >> 1)) * 1024;
#pragma unroll
      for (int m = 0; m < 3; ++m) st16(pz, rWS, lane * 16 + m * 1024, ao);
      __builtin_amdgcn_raw_buffer_store_b64(v2u{POISON, POISON}, rWS, lane * 16 + (j & 1) * 8, ao + ((j & 1) ? -1024 : 3072), SC1);
      asm volatile("" ::: "memory");
    }
  }
  STAMP(2);
  const float mean1 = rowsum4(s0 + s1) * (1.f / 512.f);
  const float rstd1 = __builtin_amdgcn_rsqf(fmaxf(rowsum4(ss0 + ss1) * (1.f / 512.f) - mean1 * mean1, 0.f) + 1e-5f);
  VMWAIT(0);
  BAR();
  STAMP(3);
  const int w1col = 224 * j + (j & 1) * 32;
  const unsigned vW1 = (lane >> 2) * 3584 + (pp ^ ((g & 1) << 1)) * 16;
  {
    const int m = w >> 2, blk0 = (w & 3) * 8;
#pragma unroll
    for (int n = 0; n < 8; ++n) dma16(dW1, vW1, (blk0 + n) * (16 * 3584) + w1col + m * 64, lds0 + m * 32768 + (blk0 + n) * 1024);
  }
  {
    float mr[4], rs[4];
#pragma unroll
    for (int r = 0; r < 4; ++r) {
      mr[r] = __builtin_bit_cast(float, __builtin_amdgcn_ds_bpermute((4 * g + r) * 4, __builtin_bit_cast(int, mean1)));
      rs[r] = __builtin_bit_cast(float, __builtin_amdgcn_ds_bpermute((4 * g + r) * 4, __builtin_bit_cast(int, rstd1)));
    }
    const LAS float* cq = (const LAS float*)(L + L_CQ);
    const float ck0 = cq[i], ck1 = cq[16 + i], cv0 = cq[32 + i], cv1 = cq[48 + i];
    float k0[4], k1[4], v0[4], v1[4];
#pragma unroll
    for (int r = 0; r < 4; ++r) {
      k0[r] = phi((ak0[r] - mr[r] * ck0) * rs[r]);
      k1[r] = phi((ak1[r] - mr[r] * ck1) * rs[r]);
      v0[r] = (av0[r] - mr[r] * cv0) * rs[r];
      v1[r] = (av1[r] - mr[r] * cv1) * rs[r];
    }
    const v2u pk0 = {pk(k0[0], k0[1]), pk(k0[2], k0[3])}, pk1 = {pk(k1[0], k1[1]), pk(k1[2], k1[3])};
    const v2u pv0 = {pk(v0[0], v0[1]), pk(v0[2], v0[3])}, pv1 = {pk(v1[0], v1[1]), pk(v1[2], v1[3])};
    const v2u one2 = {0x3F803F80u, 0x3F803F80u};
    const f4 z4 = {0, 0, 0, 0};
    const f4 kv0 = mfma16(pk0, pv0, z4), kv1 = mfma16(pk1, pv1, z4), z0 = mfma16(pk0, one2, z4), z1 = mfma16(pk1, one2, z4);
    *(LAS f4*)(L + L_KVRED + (w * 2 + 0) * 1024 + lane * 16) = kv0;
    *(LAS f4*)(L + L_KVRED + (w * 2 + 1) * 1024 + lane * 16) = kv1;
    if (i < 2) *(LAS f4*)(L + L_ZP + (w * 2 + i) * 64 + g * 16) = i ? z1 : z0;
  }
  BAR();
  STAMP(4);
  if (w == 0) {
    f4 a = {0, 0, 0, 0}, b = a, z = a;
#pragma unroll
    for (int ww = 0; ww < 8; ++ww) {
      a += *(LAS f4*)(L + L_KVRED + (ww * 2 + 0) * 1024 + lane * 16);
      b += *(LAS f4*)(L + L_KVRED + (ww * 2 + 1) * 1024 + lane * 16);
      z += *(LAS f4*)(L + L_ZP + ww * 128 + (lane & 7) * 16);
    }
    const int so = WS_KV + (j * 16 + p) * KV_REC;
    st16(__builtin_bit_cast(v4u, a), rWS, lane * 16, so);
    st16(__builtin_bit_cast(v4u, b), rWS, lane * 16 + 1024, so);
    if (lane < 8) st16(__builtin_bit_cast(v4u, z), rWS, lane * 16 + 2048, so);
  }
  f4 aq0 = {0, 0, 0, 0}, aq1 = aq0;
#pragma unroll
  for (int s = 0; s < 16; ++s) {
    const int off = L_WQ + (128 * (s >> 2) + 8 * (s & 3)) * 64;
    aq0 = mfma32(trfrag(tX0 + off, 256), xf[s], aq0);
    aq1 = mfma32(trfrag(tX1 + off, 256), xf[s], aq1);
  }
  const unsigned tag = epoch + 1u;
  GAS unsigned* const flags = (GAS unsigned*)(ws + WS_FLAG);
  if (w == 0) {
    VMWAIT(0);
    if (lane == 0) __hip_atomic_store(flags + (p * 16 + j) * 32, tag, RLX_AGENT);
    STAMP(5);
    const int fi = (lane & 16) ? (p * 16 + i) : (i * 16 + j);
    for (unsigned sp = 0; sp < (1u << 15); ++sp) {
      const unsigned v = lane < 32 ? __hip_atomic_load(flags + fi * 32, RLX_AGENT) : tag;
      if (__all(v == tag)) break;
      COUNT(18);
    }
  } else if (w == 4 || w == 5) {
    LAS char* base = ((w & 1) ? tX1 : tX0) + L_WQ;
    const v4u ones = {0x3F803F80u, 0x3F803F80u, 0x3F803F80u, 0x3F803F80u};
    f4 c = {0.f, 0.f, 0.f, 0.f};
#pragma unroll 4
    for (int s = 0; s < 16; ++s) c = mfma32(ones, trfrag(base + ((s >> 2) * 128 + (s & 3) * 8) * 64, 256), c);
    if (g == 0) *(LAS float*)(L + L_CQ + (w * 16 + i) * 4) = c[0];
  }
  BAR();
  STAMP(6);
  v2u pq0, pq1;
  {
    const int so = WS_KV + (j * 16 + 4 * (w >> 1)) * KV_REC;
    v4u t[4];
#pragma unroll
    for (int n = 0; n < 4; ++n) t[n] = __builtin_amdgcn_raw_buffer_load_b128(rWS, lane * 16 + (w & 1) * 1024, so + n * KV_REC, SC1);
    const v4u zt = __builtin_amdgcn_raw_buffer_load_b128(rWS, pp * KV_REC + 2048 + (w & 1) * 64 + g * 16, so, SC1);
    const f4 c0 = *(LAS f4*)(L + L_CQ + (64 + 4 * g) * 4), c1 = *(LAS f4*)(L + L_CQ + (80 + 4 * g) * 4);
    float a[4], b[4];
#pragma unroll
    for (int r = 0; r < 4; ++r) {
      a[r] = phi((aq0[r] - mean1 * c0[r]) * rstd1);
      b[r] = phi((aq1[r] - mean1 * c1[r]) * rstd1);
    }
    pq0 = v2u{pk(a[0], a[1]), pk(a[2], a[3])};
    pq1 = v2u{pk(b[0], b[1]), pk(b[2], b[3])};
    const f4 s = (__builtin_bit_cast(f4, t[0]) + __builtin_bit_cast(f4, t[1])) + (__builtin_bit_cast(f4, t[2]) + __builtin_bit_cast(f4, t[3]));
    *(LAS f4*)(L + L_CONS + w * 1024 + lane * 16) = s;
    f4 z = __builtin_bit_cast(f4, zt);
#pragma unroll
    for (int r = 0; r < 4; ++r) {
      z[r] += asf(__builtin_amdgcn_mov_dpp(asu(z[r]), 0xB1, 0xf, 0xf, true));
      z[r] += asf(__builtin_amdgcn_mov_dpp(asu(z[r]), 0x4E, 0xf, 0xf, true));
    }
    if (i == 0) *(LAS f4*)(L + L_ZC + w * 64 + g * 16) = z;
  }
  BAR();
  STAMP(7);
  {
#pragma unroll
    for (int n = 0; n < 4; ++n) dma16(dW1, vW1, (4 * w + n) * (16 * 3584) + w1col + 128, lds0 + L_WQ + (4 * w + n) * 1024);
    const unsigned vs = (lane >> 1) * 3584 + (lane & 1) * 16;
#pragma unroll
    for (int n = 0; n < 2; ++n) dma16(dW1, vs, (2 * w + n) * (32 * 3584) + 224 * j + ((j & 1) ? 0 : 192), lds0 + L_W1S + (2 * w + n) * 1024);
  }
  {
    f4 k0 = {0, 0, 0, 0}, k1 = k0, z0 = k0, z1 = k0;
#pragma unroll
    for (int n = 0; n < 4; ++n) {
      k0 += *(LAS f4*)(L + L_CONS + (2 * n) * 1024 + lane * 16);
      k1 += *(LAS f4*)(L + L_CONS + (2 * n + 1) * 1024 + lane * 16);
      z0 += *(LAS f4*)(L + L_ZC + (2 * n) * 64 + g * 16);
      z1 += *(LAS f4*)(L + L_ZC + (2 * n + 1) * 64 + g * 16);
    }
    const f4 z4 = {0, 0, 0, 0};
    const f4 n0 = mfma16(v2u{pk(k0[0], k0[1]), pk(k0[2], k0[3])}, pq0, z4), n1 = mfma16(v2u{pk(k1[0], k1[1]), pk(k1[2], k1[3])}, pq1, z4);
    const f4 d0 = mfma16(v2u{pk(z0[0], z0[1]), pk(z0[2], z0[3])}, pq0, z4), d1 = mfma16(v2u{pk(z1[0], z1[1]), pk(z1[2], z1[3])}, pq1, z4);
    const float r0 = __builtin_amdgcn_rcpf(d0[0] + 1e-6f), r1 = __builtin_amdgcn_rcpf(d1[0] + 1e-6f);
    const v4u o = {nopoison(pk(n0[0] * r0, n0[1] * r0)), nopoison(pk(n0[2] * r0, n0[3] * r0)), nopoison(pk(n1[0] * r1, n1[1] * r1)), nopoison(pk(n1[2] * r1, n1[3] * r1))};
    st16(o, rWS, lane * 16, WS_O + ((p * 8 + w) * 16 + j) * 1024);
  }
  VMWAIT(0);
  BAR();
  STAMP(8);
  {
    f4 a0, a1;
    const int so = WS_O + (p * 8 + w) * 16384;
    for (unsigned tries = 0; tries < 4096; ++tries) {
      v4u f[16];
#pragma unroll
      for (int s = 0; s < 16; ++s) f[s] = __builtin_amdgcn_raw_buffer_load_b128(rWS, lane * 16 + (s & 3) * 1024, so + (s >> 2) * 4096, SC1);
      __builtin_amdgcn_sched_barrier(0);
      a0 = f4{0, 0, 0, 0}; a1 = a0;
      unsigned m = 0;
#pragma unroll
      for (int s = 0; s < 16; ++s) {
        a0 = mfma32(trfrag(tC0 + L_WO + s * 2048, 1024), f[s], a0);
        a1 = mfma32(trfrag(tC1 + L_WO + s * 2048, 1024), f[s], a1);
        m = fragmax(m, f[s]);
      }
      if (__all(m != POISON)) break;
      COUNT(19); __builtin_amdgcn_s_sleep(4);
      asm volatile("" ::: "memory");
    }
    const v4u h = {nopoison(pk(a0[0] + bflo(xr0.x), a0[1] + bfhi(xr0.x))), nopoison(pk(a0[2] + bflo(xr0.y), a0[3] + bfhi(xr0.y))),
                   nopoison(pk(a1[0] + bflo(xr1.x), a1[1] + bfhi(xr1.x))), nopoison(pk(a1[2] + bflo(xr1.y), a1[3] + bfhi(xr1.y)))};
    st16(h, rWS, lane * 16, WS_H + ((p * 8 + w) * 16 + j) * 1024);
    if (bid == 0 && tid == 0) __builtin_amdgcn_raw_buffer_store_b32(tag, rWS, WS_EPOCH, 0, SC1);
  }
  STAMP(9);
  VMWAIT(0);
  BAR();
  STAMP(10);
  const int th = j >> 3, c4 = j & 7;
  v2u w2r[56];
  v2u hr0, hr1;
  {
    v4u f[16];
    float mean2, rstd2;
    const int so = WS_H + (p * 8 + w) * 16384;
    f4 a0, a1;
    for (unsigned tries = 0; tries < 4096; ++tries) {
#pragma unroll
      for (int s = 0; s < 16; ++s) f[s] = __builtin_amdgcn_raw_buffer_load_b128(rWS, lane * 16 + (s & 3) * 1024, so + (s >> 2) * 4096, SC1);
      __builtin_amdgcn_sched_barrier(0);
      if (tries == 0 && w < 7) {
        float c;
        if (w < 6) c = colsum_tile(((w & 1) ? tC1 : tC0) + (w >> 1) * 32768, 2048, 1024);
        else c = colsum_tile(L + L_W1S + (4 * g + q) * 32 + pp * 8, 1024, 512);
        const int col = 112 * j + (w < 6 ? (j & 1) * 16 + w * 16 : ((j & 1) ? 0 : 96)) + i;
        if (g == 0) {
          *(LAS float*)(L + L_C1 + (w * 16 + i) * 4) = c;
          *(LAS float*)(L + L_B1 + (w * 16 + i) * 4) = bflo(b1[col]);
        }
      }
      a0 = f4{0, 0, 0, 0}; a1 = a0;
      unsigned m = 0;
      float t0 = 0.f, t1 = 0.f, tt0 = 0.f, tt1 = 0.f;
#pragma unroll
      for (int s = 0; s < 16; ++s) {
        const v4u a = f[s];
        a0 = mfma32(trfrag(tC0 + s * 2048, 1024), a, a0);
        a1 = mfma32(trfrag(tC1 + s * 2048, 1024), a, a1);
        m = fragmax(m, a);
        t0 = dot2(a.x, 0x3F803F80u, t0); t1 = dot2(a.y, 0x3F803F80u, t1); t0 = dot2(a.z, 0x3F803F80u, t0); t1 = dot2(a.w, 0x3F803F80u, t1);
        tt0 = dot2(a.x, a.x, tt0); tt1 = dot2(a.y, a.y, tt1); tt0 = dot2(a.z, a.z, tt0); tt1 = dot2(a.w, a.w, tt1);
      }
      mean2 = rowsum4(t0 + t1) * (1.f / 512.f);
      rstd2 = __builtin_amdgcn_rsqf(fmaxf(rowsum4(tt0 + tt1) * (1.f / 512.f) - mean2 * mean2, 0.f) + 1e-5f);
      if (__all(m != POISON)) break;
      COUNT(20); __builtin_amdgcn_s_sleep(4);
      asm volatile("" ::: "memory");
    }
    STAMP(11);
    BAR();
    STAMP(12);
    {
      const int vo = g * 4096 + i * 8;
#pragma unroll
      for (int s = 0; s < 7; ++s)
#pragma unroll
        for (int e = 0; e < 8; ++e)
          w2r[s * 8 + e] = __builtin_amdgcn_raw_buffer_load_b64(rW2, vo + (e & 3) * 1024, (32 * (7 * w + s) + 16 * (e >> 2)) * 1024 + c4 * 128, 0);
      const int tok = tid >> 3, n0 = 64 * c4 + 8 * (tid & 7);
      const int ho = WS_H + ((p * 8 + 4 * th + (tok >> 4)) * 16 + (n0 >> 5)) * 1024 + (16 * ((n0 & 15) >> 2) + (tok & 15)) * 16 + ((n0 >> 4) & 1) * 8;
      hr0 = __builtin_amdgcn_raw_buffer_load_b64(rWS, ho, 0, SC1);
      hr1 = __builtin_amdgcn_raw_buffer_load_b64(rWS, ho + 256, 0, SC1);
      __builtin_amdgcn_sched_barrier(0);
    }
    const float nm = -mean2 * rstd2;
    const int ao = WS_A + ((p * 8 + w) * 56 + ((7 * j + (j & 1)) >> 1)) * 1024;
#pragma unroll
    for (int m = 0; m < 4; ++m) {
      f4 n0 = {0, 0, 0, 0}, n1 = n0;
      if (m < 2) {
#pragma unroll
        for (int s = 0; s < 16; ++s) {
          n0 = mfma32(trfrag(tC0 + (m + 1) * 32768 + s * 2048, 1024), f[s], n0);
          n1 = mfma32(trfrag(tC1 + (m + 1) * 32768 + s * 2048, 1024), f[s], n1);
        }
      } else if (m == 2) {
#pragma unroll
        for (int s = 0; s < 16; ++s) n0 = mfma32(trfrag(L + L_W1S + (4 * g + q) * 32 + pp * 8 + s * 1024, 512), f[s], n0);
      }
      const f4 c0 = *(LAS f4*)(L + L_C1 + (m * 32 + 4 * g) * 4), bb0 = *(LAS f4*)(L + L_B1 + (m * 32 + 4 * g) * 4);
      float y[4];
#pragma unroll
      for (int r = 0; r < 4; ++r) y[r] = mish(a0[r] * rstd2 + (nm * c0[r] + bb0[r]));
      const unsigned e0 = nopoison(pk(y[0], y[1])), e1 = nopoison(pk(y[2], y[3]));
      if (m < 3) {
        const f4 c1 = *(LAS f4*)(L + L_C1 + (m * 32 + 16 + 4 * g) * 4), bb1 = *(LAS f4*)(L + L_B1 + (m * 32 + 16 + 4 * g) * 4);
#pragma unroll
        for (int r = 0; r < 4; ++r) y[r] = mish(a1[r] * rstd2 + (nm * c1[r] + bb1[r]));
        st16(v4u{e0, e1, nopoison(pk(y[0], y[1])), nopoison(pk(y[2], y[3]))}, rWS, lane * 16 + m * 1024, ao);
      } else {
        __builtin_amdgcn_raw_buffer_store_b64(v2u{e0, e1}, rWS, lane * 16 + (j & 1) * 8, ao + ((j & 1) ? -1024 : 3072), SC1);
      }
      a0 = n0; a1 = n1;
    }
  }
  STAMP(13);
  VMWAIT(0);
  BAR();
  STAMP(14);
  {
    f4 acc[4][4];
#pragma unroll
    for (int t = 0; t < 4; ++t)
#pragma unroll
      for (int u = 0; u < 4; ++u) acc[t][u] = f4{0, 0, 0, 0};
    const int so = WS_A + ((p * 8 + 4 * th) * 56 + 7 * w) * 1024;
    constexpr int ord[7] = {0, 4, 1, 5, 2, 6, 3};
    v4u f[7][4];
#pragma unroll
    for (int n = 0; n < 3; ++n)
#pragma unroll
      for (int t = 0; t < 4; ++t) f[n][t] = __builtin_amdgcn_raw_buffer_load_b128(rWS, lane * 16, so + (t * 56 + ord[n]) * 1024, SC1);
    __builtin_amdgcn_sched_barrier(0);
#pragma unroll
    for (int n = 0; n < 7; ++n) {
      const int s = ord[n];
      v4u wf[4];
#pragma unroll
      for (int u = 0; u < 4; ++u)
#pragma unroll
        for (int m = 0; m < 4; ++m)
          wf[u][m] = __builtin_amdgcn_perm(w2r[s * 8 + 2 * m + 1][u >> 1], w2r[s * 8 + 2 * m][u >> 1], (u & 1) ? 0x07060302u : 0x05040100u);
      for (unsigned tries = 0; tries < 4096; ++tries) {
        const unsigned m = fragmax(fragmax(fragmax(fragmax(0u, f[n][0]), f[n][1]), f[n][2]), f[n][3]);
        if (__all(m != POISON)) break;
        COUNT(21); __builtin_amdgcn_s_sleep(4);
        asm volatile("" ::: "memory");
#pragma unroll
        for (int t = 0; t < 4; ++t) f[n][t] = __builtin_amdgcn_raw_buffer_load_b128(rWS, lane * 16, so + (t * 56 + s) * 1024, SC1);
      }
      if (n + 3 < 7) {
#pragma unroll
        for (int t = 0; t < 4; ++t) f[n + 3][t] = __builtin_amdgcn_raw_buffer_load_b128(rWS, lane * 16, so + (t * 56 + ord[n + 3 < 7 ? n + 3 : 0]) * 1024, SC1);
      }
#pragma unroll
      for (int t = 0; t < 4; ++t)
#pragma unroll
        for (int u = 0; u < 4; ++u) acc[t][u] = mfma32(wf[u], f[n][t], acc[t][u]);
      __builtin_amdgcn_sched_barrier(0);
    }
    STAMP(15);
#pragma unroll
    for (int t = 0; t < 4; ++t)
#pragma unroll
      for (int u = 0; u < 4; ++u) *(LAS f4*)(L + ((w * 16 + t * 4 + u) * 64 + 16 * g + (i ^ (4 * g))) * 16) = acc[t][u];
    BAR();
    STAMP(16);
    const int tok = tid >> 3, pc = tid & 7, gg = pc >> 1;
    LAS char* rp = L + (((tok >> 4) * 4) * 64 + 16 * gg + ((tok & 15) ^ (4 * gg))) * 16 + (pc & 1) * 8;
    f2 r[4];
#pragma unroll
    for (int u = 0; u < 4; ++u) {
      r[u] = *(LAS f2*)(rp + u * 1024);
#pragma unroll
      for (int ww = 1; ww < 8; ++ww) r[u] += *(LAS f2*)(rp + ww * 16384 + u * 1024);
    }
    const v4u o = {pk(r[0][0] + bflo(hr0.x), r[1][0] + bfhi(hr0.x)), pk(r[2][0] + bflo(hr0.y), r[3][0] + bfhi(hr0.y)),
                   pk(r[0][1] + bflo(hr1.x), r[1][1] + bfhi(hr1.x)), pk(r[2][1] + bflo(hr1.y), r[3][1] + bfhi(hr1.y))};
    *(v4u*)(out + (size_t)(p * 128 + th * 64 + tok) * 512 + 64 * c4 + 8 * pc) = o;
  }
  STAMP(17);
  STAMP_FLUSH
}

extern "C" void kernel_launch(void* const* d_in, const int* in_sizes, int n_in, void* d_out, int out_size, void* d_ws, size_t ws_size,
                              hipStream_t stream) {
  (void)in_sizes; (void)n_in; (void)out_size; (void)ws_size;
  (void)hipFuncSetAttribute((const void*)fused_block, hipFuncAttributeMaxDynamicSharedMemorySize, LDS_BYTES);
  fused_block<<<256, 512, LDS_BYTES, stream>>>((const u16*)d_in[0], (const u16*)d_in[1], (const u16*)d_in[2], (const u16*)d_in[4],
                                               (const u16*)d_in[5], (const u16*)d_in[6], (u16*)d_out, (char*)d_ws);
}
```

```cpp
#include <hip/hip_runtime.h>
#include <hip/hip_bf16.h>

typedef unsigned short u16;
typedef unsigned v4u __attribute__((ext_vector_type(4)));
typedef unsigned v2u __attribute__((ext_vector_type(2)));
typedef float f4 __attribute__((ext_vector_type(4)));
typedef float f2 __attribute__((ext_vector_type(2)));
typedef short s4 __attribute__((ext_vector_type(4)));
typedef __bf16 b8 __attribute__((ext_vector_type(8)));
typedef __bf16 b2 __attribute__((ext_vector_type(2)));
#define GAS __attribute__((address_space(1)))
#define LAS __attribute__((address_space(3)))
#define DI __device__ __forceinline__
#define RLX_AGENT __ATOMIC_RELAXED, __HIP_MEMORY_SCOPE_AGENT

#define MAPMODE 0
#define TOUCH 1
#define TOUCH2 1
#define QF 5
#define ROT 1
#define PD 6
#define W2B 2
#define W2C 3

constexpr int CODE_KB = 24;
constexpr int WS_EPOCH = 0;
constexpr int WS_FLAG = 4096;
constexpr int WS_KV = 131072;
constexpr int KV_REC = 2304;
constexpr int WS_O = 1 << 20;
constexpr int WS_H = 3 << 20;
constexpr int WS_A = 5 << 20;
constexpr int WS_BYTES = 12 << 20;
#define STAMP(n)
#define STAMPX(n)
#define STAMP_DECL
#define STAMP_FLUSH
#define COUNT(n)

constexpr int L_WQ = 0, L_WK = 32768, L_WV = 65536, L_KVRED = 98304, L_WO = 114688, L_SCR = 147456;
constexpr int L_W1 = 0;
constexpr int L_CONS = L_SCR;
constexpr int L_ZP = L_SCR + 8192;
constexpr int L_ZC = L_ZP + 1024;
constexpr int L_CKV = L_ZC + 512;
constexpr int L_CQ = L_CKV + 2048;
constexpr int L_C1 = L_CQ + 128;
constexpr int L_B1 = L_C1 + 512;
constexpr int L_HRES = 131072;
constexpr int LDS_BYTES = 163840;

constexpr unsigned POISON = 0xFFFFFFFFu;
constexpr unsigned ONE2 = 0x3F803F80u;
constexpr int SC1 = 16;
constexpr float LOG2E = 1.4426950408889634f;

DI float asf(unsigned u) { return __builtin_bit_cast(float, u); }
DI unsigned asu(float f) { return __builtin_bit_cast(unsigned, f); }
DI f4 mfma32(v4u a, v4u b, f4 c) { return __builtin_amdgcn_mfma_f32_16x16x32_bf16(__builtin_bit_cast(b8, a), __builtin_bit_cast(b8, b), c, 0, 0, 0); }
DI f4 mfma16(v2u a, v2u b, f4 c) { return __builtin_amdgcn_mfma_f32_16x16x16bf16_1k(__builtin_bit_cast(s4, a), __builtin_bit_cast(s4, b), c, 0, 0, 0); }
DI unsigned pk(float lo, float hi) { f2 v = {lo, hi}; return __builtin_bit_cast(unsigned, __builtin_convertvector(v, b2)); }
DI float bflo(unsigned u) { return asf(u << 16); }
DI float bfhi(unsigned u) { return asf(u & 0xffff0000u); }
DI unsigned nopoison(unsigned u) { return u < 0xFFFFFFFEu ? u : 0xFFFFFFFEu; }
DI v2u trd(LAS char* p) { return __builtin_bit_cast(v2u, __builtin_amdgcn_ds_read_tr16_b64_v4i16((LAS s4*)p)); }
DI v4u trfrag(LAS char* p, int second) { v2u a = trd(p), b = trd(p + second); return v4u{a.x, a.y, b.x, b.y}; }
DI v4u ldf(LAS char* p) { return *(LAS v4u*)p; }
DI float dot2(unsigned a, unsigned b, float c) { return __builtin_amdgcn_fdot2_f32_bf16(__builtin_bit_cast(b2, a), __builtin_bit_cast(b2, b), c, false); }
DI float rowsum4(float v) {
  auto r = __builtin_amdgcn_permlane32_swap(asu(v), asu(v), false, false);
  const float s = asf(r[0]) + asf(r[1]);
  auto t = __builtin_amdgcn_permlane16_swap(asu(s), asu(s), false, false);
  return asf(t[0]) + asf(t[1]);
}
DI v4u mksrd(const void* p, unsigned bytes) {
  unsigned long long a = (unsigned long long)p;
  return v4u{(unsigned)a, (unsigned)(a >> 32) & 0xffffu, bytes, 0x00020000u};
}
DI void dma16(v4u srd, unsigned voff, unsigned soff, unsigned lds) {
  asm volatile("s_mov_b32 m0, %3\n\ts_nop 0\n\tbuffer_load_dwordx4 %0, %1, %2 offen lds" ::"v"(voff), "s"(srd), "s"(soff), "s"(lds) : "memory");
}
DI void dma4(v4u srd, unsigned voff, unsigned s0, int step, unsigned lds) {
  asm volatile("s_mov_b32 m0, %6\n\ts_nop 0\n\tbuffer_load_dwordx4 %0, %1, %2 offen lds\n\tbuffer_load_dwordx4 %0, %1, %3 offen offset:1024 lds\n\t"
               "buffer_load_dwordx4 %0, %1, %4 offen offset:2048 lds\n\tbuffer_load_dwordx4 %0, %1, %5 offen offset:3072 lds"
               ::"v"(voff), "s"(srd), "s"(s0), "s"(s0 + step - 1024), "s"(s0 + 2 * step - 2048), "s"(s0 + 3 * step - 3072), "s"(lds) : "memory");
}
DI void dma2(v4u srd, unsigned voff, unsigned s0, int step, unsigned lds) {
  asm volatile("s_mov_b32 m0, %4\n\ts_nop 0\n\tbuffer_load_dwordx4 %0, %1, %2 offen lds\n\tbuffer_load_dwordx4 %0, %1, %3 offen offset:1024 lds"
               ::"v"(voff), "s"(srd), "s"(s0), "s"(s0 + step - 1024), "s"(lds) : "memory");
}
DI void dma7(v4u srd, const unsigned (&v)[7], unsigned so, unsigned lds) {
  asm volatile("s_mov_b32 m0, %9\n\ts_nop 0\n\tbuffer_load_dwordx4 %0, %7, %8 offen lds\n\tbuffer_load_dwordx4 %1, %7, %8 offen offset:1024 lds\n\t"
               "buffer_load_dwordx4 %2, %7, %8 offen offset:2048 lds\n\tbuffer_load_dwordx4 %3, %7, %8 offen offset:3072 lds\n\t"
               "s_mov_b32 m0, %10\n\ts_nop 0\n\tbuffer_load_dwordx4 %4, %7, %8 offen lds\n\tbuffer_load_dwordx4 %5, %7, %8 offen offset:1024 lds\n\t"
               "buffer_load_dwordx4 %6, %7, %8 offen offset:2048 lds"
               ::"v"(v[0]), "v"(v[1]), "v"(v[2]), "v"(v[3]), "v"(v[4]), "v"(v[5]), "v"(v[6]), "s"(srd), "s"(so), "s"(lds), "s"(lds + 4096) : "memory");
}
DI void st16(v4u v, __amdgpu_buffer_rsrc_t r, int voff, int soff) {
  __builtin_amdgcn_raw_buffer_store_b128(v, r, voff, soff, SC1);
  asm volatile("s_nop 1" ::"v"(v) : "memory");
}
DI LAS char* opq(LAS char* p) { asm volatile("" : "+v"(p)); return p; }
#define LGKM0() asm volatile("s_waitcnt lgkmcnt(0)" ::: "memory")
#define BAR() do { LGKM0(); __builtin_amdgcn_s_barrier(); asm volatile("" ::: "memory"); } while (0)
#define VMWAIT(n) asm volatile("s_waitcnt vmcnt(%0)" ::"i"(n) : "memory")
#define SB() __builtin_amdgcn_sched_barrier(0)
DI unsigned umax3(unsigned a, unsigned b, unsigned c) { return max(max(a, b), c); }
DI unsigned fragmax(unsigned m, v4u f) { return umax3(umax3(m, f.x, f.y), f.z, f.w); }

template <int NB> DI void repack64(LAS char* blk, int rdoff, int second, int lane) {
  v4u t[2 * NB];
#pragma unroll
  for (int n = 0; n < NB; ++n) { t[2 * n] = trfrag(blk + n * 2048 + rdoff, second); t[2 * n + 1] = trfrag(blk + n * 2048 + rdoff + 32, second); }
  LGKM0();
#pragma unroll
  for (int n = 0; n < 2 * NB; ++n) *(LAS v4u*)(blk + n * 1024 + lane * 16) = t[n];
}
DI void repack224(LAS char* blk, int rdoff, int lane) {
  v4u t[7];
#pragma unroll
  for (int n = 0; n < 7; ++n) t[n] = trfrag(blk + rdoff + n * 32, 16 * 224);
  LGKM0();
#pragma unroll
  for (int n = 0; n < 7; ++n) *(LAS v4u*)(blk + n * 1024 + lane * 16) = t[n];
}
DI float colsum(LAS char* p, int step) {
  const v4u ones = {ONE2, ONE2, ONE2, ONE2};
  f4 c = {0.f, 0.f, 0.f, 0.f};
  v4u t[16];
#pragma unroll
  for (int h = 0; h < 2; ++h) {
    LAS char* const b = opq(p + h * 8 * step);
#pragma unroll
    for (int s = 0; s < 8; ++s) t[h * 8 + s] = ldf(b + s * step);
  }
  SB();
#pragma unroll
  for (int s = 0; s < 16; ++s) c = mfma32(ones, t[s], c);
  return c[0];
}
DI float phi(float t) { return fmaxf(t, 0.f) + __builtin_amdgcn_exp2f(fminf(t, 0.f) * LOG2E) + 1e-6f; }
DI float mish(float t) {
  const float n = __builtin_amdgcn_exp2f(fminf(t, 30.f) * LOG2E);
  const float u = n * (n + 2.f);
  return t * u * __builtin_amdgcn_rcpf(u + 2.f);
}
#define STATS(a) do { s0 = dot2(a.x, ONE2, s0); s1 = dot2(a.y, ONE2, s1); s0 = dot2(a.z, ONE2, s0); s1 = dot2(a.w, ONE2, s1); \
                      ss0 = dot2(a.x, a.x, ss0); ss1 = dot2(a.y, a.y, ss1); ss0 = dot2(a.z, a.z, ss0); ss1 = dot2(a.w, a.w, ss1); } while (0)

DI void w2pack(v4u (&r)[4]) {
  v2u lo[4], hi[4];
#pragma unroll
  for (int n = 0; n < 4; ++n)
#pragma unroll
    for (int d = 0; d < 2; ++d) {
      const unsigned a = r[n][d], b = r[n][2 + d];
      lo[n][d] = __builtin_amdgcn_update_dpp(a, b, 0x128, 0xf, 0xc, false);
      hi[n][d] = __builtin_amdgcn_update_dpp(b, a, 0x128, 0xf, 0x3, false);
    }
#pragma unroll
  for (int u = 0; u < 4; ++u) {
    const unsigned sel = (u & 1) ? 0x07060302u : 0x05040100u;
    r[u] = v4u{__builtin_amdgcn_perm(lo[1][u >> 1], lo[0][u >> 1], sel), __builtin_amdgcn_perm(lo[3][u >> 1], lo[2][u >> 1], sel),
               __builtin_amdgcn_perm(hi[1][u >> 1], hi[0][u >> 1], sel), __builtin_amdgcn_perm(hi[3][u >> 1], hi[2][u >> 1], sel)};
  }
}

__global__ void __launch_bounds__(512, 2)
fused_block(const u16* __restrict__ x, const u16* __restrict__ wqkv, const u16* __restrict__ wo, const u16* __restrict__ w1,
            const u16* __restrict__ b1, const u16* __restrict__ w2, u16* __restrict__ out, char* ws) {
  extern __shared__ __attribute__((aligned(16))) char smem_[];
  LAS char* L = (LAS char*)smem_;
  const int tid = threadIdx.x, lane = tid & 63, w = __builtin_amdgcn_readfirstlane(tid >> 6);
  const int g = lane >> 4, i = lane & 15, q = (lane >> 2) & 3, pp = lane & 3;
  const int bid = blockIdx.x, xcd = bid & 7, rk = bid >> 3;
  const int p = 2 * xcd + (rk >> 4), j = rk & 15;
  const int rot = ROT ? 2 * (j & 1) + 4 * (p & 1) + (j >> 1) : 0;
  STAMP_DECL
  STAMP(0);
  const v4u dQKV = mksrd(wqkv, 512 * 1536 * 2), dWO = mksrd(wo, 512 * 512 * 2), dW1 = mksrd(w1, 512 * 1792 * 2);
  const v4u dX = mksrd((const char*)x - 4096, 2048 * 512 * 2 + 4096);
  const __amdgpu_buffer_rsrc_t rWS = __builtin_amdgcn_make_buffer_rsrc(ws, 0, WS_BYTES, 0x00020000);
  const __amdgpu_buffer_rsrc_t rW2 = __builtin_amdgcn_make_buffer_rsrc((void*)w2, 0, 1792 * 512 * 2, 0x00020000);
  const unsigned lds0 = (unsigned)(size_t)L;
  LAS char* const Ll = L + lane * 16;

  unsigned tch = 0;
  {
    unsigned long long pc;
    asm volatile("s_getpc_b64 %0" : "=s"(pc));
    const unsigned long long ta = w == 0 ? (unsigned long long)x + bid * 8192 : w == 1 ? (unsigned long long)wqkv + bid * 6144 : (pc & ~127ull) + rk * 1024;
    const int nl = w == 0 ? 64 : w == 1 ? 48 : (w == 2 && rk < CODE_KB) ? 8 : 0;
    if (lane < nl) asm volatile("global_load_dword %0, %1, %2" : "+v"(tch) : "v"(lane * 128), "s"(ta) : "memory");
    SB();
  }
  const unsigned sw8 = (pp ^ ((lane >> 5) << 1)) * 16;
  const unsigned vW = (lane >> 2) * 3072 + sw8;
  const unsigned vX0 = (lane >> 3) * 1024 + ((lane & 7) ^ g) * 16, vX1 = (vX0 ^ 64) + 8192;
  const unsigned sX = (p * 128 + w * 16) * 1024 + 4096;
  const unsigned sW = (w & 3) * (16 * 3072) + (1 + (w >> 2)) * 1024 + j * 64, lW = lds0 + L_WK + (w >> 2) * 32768 + (w & 3) * 1024;
#define XSLOT(c) ((c) % 3 == 0 ? L_KVRED + w * 2048 : L_WO + w * 4096 + ((c) % 3 - 1) * 2048)
#define WLOAD(c) dma16(dQKV, vW, sW + (((c) + rot) & 7) * (64 * 3072), lW + (c) * 4096)
#define XLOAD1(c, h) dma16(dX, (h) ? vX1 : vX0, sX + (((c) + rot) & 7) * 128, lds0 + XSLOT(c) + (h) * 1024)
#define XLOAD(c) do { XLOAD1(c, 0); XLOAD1(c, 1); } while (0)
#define QLOAD1(n) dma16(dQKV, vW, (((w + rot) & 7) * 64 + (n) * 16) * 3072 + j * 64, lds0 + L_WQ + w * 4096 + (n) * 1024)
  unsigned epoch;
  asm volatile("global_load_dword %0, %1, %2 sc1" : "=v"(epoch) : "v"(0), "s"(ws) : "memory");
  WLOAD(0); XLOAD(0);
  __builtin_amdgcn_s_barrier();
  WLOAD(1); XLOAD(1);
  STAMP(1);
  LAS char* const tN0 = L + (8 * g + q) * 64 + pp * 8 + (g & 1) * 32;
  LAS char* const tN1 = L + (8 * g + q) * 64 + pp * 8 + ((g & 1) ^ 1) * 32;
  LAS char* const kN0 = opq(tN0 + L_WK);
  LAS char* const kN1 = opq(tN1 + L_WK);
  LAS char* const kC = opq(((w & 1) ? tN1 : tN0) + L_WK + ((w >> 1) & 1) * 32768 + (w >> 2) * 2048);
#define WFRAG(s, t) trfrag((((t) & 1) ? kN1 : kN0) + ((t) >> 1) * 32768 + (s) * 2048, 256)
  v4u ones = {ONE2, ONE2, ONE2, ONE2};
  asm volatile("" : "+v"(ones));
  v4u xf[16];
  v2u xr0, xr1;
  f4 ak0 = {0, 0, 0, 0}, ak1 = ak0, av0 = ak0, av1 = ak0, sm = ak0, gr = ak0, cs = ak0;
  const int xrd = i * 128 + (g ^ (i >> 1)) * 16;
  LAS char* const xa0 = L + L_KVRED + w * 2048 + xrd;
  LAS char* const xa1 = L + L_KVRED + w * 2048 + (xrd ^ 64);
  LAS char* const xb0 = L + L_WO + w * 4096 + xrd;
  LAS char* const xb1 = L + L_WO + w * 4096 + (xrd ^ 64);
#define XREAD(c) do { xf[2 * (c)] = ldf((c) % 3 ? xb0 + ((c) % 3 - 1) * 2048 : xa0); xf[2 * (c) + 1] = ldf((c) % 3 ? xb1 + ((c) % 3 - 1) * 2048 : xa1); } while (0)
  v4u wf[2][8], cf;
  f2 cp2[4];
  const int cres = ((j >> 1) - rot) & 7;
#pragma unroll
  for (int c = -1; c < 8; ++c) {
    if (c >= 0) {
      sm = mfma32(xf[2 * c], ones, sm);
      gr = mfma32(xf[2 * c], xf[2 * c], gr);
      SB();
    }
    if (c < 7) VMWAIT(c < 6 ? 3 : 2); else LGKM0();
    __builtin_amdgcn_s_barrier();
    asm volatile("" ::: "memory");
    STAMPX(11 + c);
    if (c < 7) { XREAD(c + 1); cf = trfrag(kC + (c + 1) * 4096, 256); }
    else {
#pragma unroll
      for (int t = 0; t < 4; ++t) cp2[t] = *(LAS f2*)(L + L_CKV + (t * 16 + i) * 8);
    }
    SB();
#pragma unroll
    for (int h = 0; h < 2; ++h) {
      if (c >= 0 && h == 1) {
        sm = mfma32(xf[2 * c + 1], ones, sm);
        gr = mfma32(xf[2 * c + 1], xf[2 * c + 1], gr);
      }
      if (h == 1) { if (c < 5) XLOAD1(c + 3, 0); else if (c < 7) QLOAD1(2 * c - 10); }
      SB();
      if (c >= 0) {
        ak0 = mfma32(xf[2 * c + h], wf[c & 1][4 * h], ak0);
        ak1 = mfma32(xf[2 * c + h], wf[c & 1][4 * h + 1], ak1);
      }
      if (c < 7) { wf[(c + 1) & 1][4 * h] = WFRAG(2 * c + 2 + h, 0); wf[(c + 1) & 1][4 * h + 1] = WFRAG(2 * c + 2 + h, 1); }
      if (h == 0) { if (c < 5) WLOAD(c + 3); }
      SB();
      if (c >= 0) {
        av0 = mfma32(xf[2 * c + h], wf[c & 1][4 * h + 2], av0);
        av1 = mfma32(xf[2 * c + h], wf[c & 1][4 * h + 3], av1);
      }
      if (c < 7) { wf[(c + 1) & 1][4 * h + 2] = WFRAG(2 * c + 2 + h, 2); wf[(c + 1) & 1][4 * h + 3] = WFRAG(2 * c + 2 + h, 3); }
      if (h == 1) { if (c < 5) XLOAD1(c + 3, 1); else if (c < 7) QLOAD1(2 * c - 9); }
      SB();
    }
    if (c < 7) {
      cs = mfma32(ones, cf, cs);
      if (c + 1 == cres) {
        LAS char* const rp = L + XSLOT(c + 1) + i * 128 + (g & 1) * 8;
        xr0 = *(LAS v2u*)(rp + (((j & 1) * 4 + (g >> 1)) ^ (i >> 1)) * 16);
        xr1 = *(LAS v2u*)(rp + (((j & 1) * 4 + (g >> 1) + 2) ^ (i >> 1)) * 16);
        LGKM0();
      }
    }
    if (c == 6) {
      const unsigned long long ta = w == 1 ? (unsigned long long)wo + bid * 2048 : w == 2 ? (unsigned long long)w1 + bid * 7168 : w == 3 ? (unsigned long long)w2 + bid * 7168
                                           : (unsigned long long)ws + WS_FLAG + (bid + (w & 3) * 256) * 128;
      const int nl = w == 0 ? 0 : w == 1 ? 16 : w < 4 ? 56 : w < 7 ? 1 : 0;
      if (lane < nl) asm volatile("global_load_dword %0, %1, %2" : "+v"(tch) : "v"(lane * 128), "s"(ta) : "memory");
      SB();
    }
    if (c == 6 && g == 0) *(LAS float*)(L + L_CKV + ((w & 3) * 16 + i) * 8 + (w >> 2) * 4) = cs[0];
  }
  STAMP(3);
  float mr[4], rs[4];
  {
    float cc[4];
#pragma unroll
    for (int t = 0; t < 4; ++t) cc[t] = cp2[t][0] + cp2[t][1];
#pragma unroll
    for (int r = 0; r < 4; ++r) {
      const float gd = gr[r];
      const float sq = asf(__builtin_amdgcn_ds_bpermute((20 * g + r) * 4, asu(gd)));
      mr[r] = sm[r] * (1.f / 512.f);
      rs[r] = __builtin_amdgcn_rsqf(fmaxf(sq * (1.f / 512.f) - mr[r] * mr[r], 0.f) + 1e-5f);
    }
    float k0[4], k1[4], v0[4], v1[4];
#pragma unroll
    for (int r = 0; r < 4; ++r) {
      k0[r] = phi((ak0[r] - mr[r] * cc[0]) * rs[r]);
      k1[r] = phi((ak1[r] - mr[r] * cc[1]) * rs[r]);
      v0[r] = (av0[r] - mr[r] * cc[2]) * rs[r];
      v1[r] = (av1[r] - mr[r] * cc[3]) * rs[r];
    }
    const v2u pk0 = {pk(k0[0], k0[1]), pk(k0[2], k0[3])}, pk1 = {pk(k1[0], k1[1]), pk(k1[2], k1[3])};
    const v2u pv0 = {pk(v0[0], v0[1]), pk(v0[2], v0[3])}, pv1 = {pk(v1[0], v1[1]), pk(v1[2], v1[3])};
    const v2u one2 = {ONE2, ONE2};
    const f4 z4 = {0, 0, 0, 0};
    const f4 kv0 = mfma16(pk0, pv0, z4), kv1 = mfma16(pk1, pv1, z4), z0 = mfma16(pk0, one2, z4), z1 = mfma16(pk1, one2, z4);
    *(LAS f4*)(Ll + L_KVRED + (w * 2 + 0) * 1024) = kv0;
    *(LAS f4*)(Ll + L_KVRED + (w * 2 + 1) * 1024) = kv1;
    if (i == 0) { *(LAS f4*)(L + L_ZP + w * 128 + g * 16) = z0; *(LAS f4*)(L + L_ZP + w * 128 + 64 + g * 16) = z1; }
  }
  if (TOUCH2 && w && w < 7) VMWAIT(1); else VMWAIT(0);
  asm volatile("" : "+v"(epoch));
  BAR();
  STAMP(4);
  const int ohoff = ((p * 8 + w) * 16 + j) * 1024;
  const unsigned vO = (lane >> 2) * 1024 + (pp ^ ((g & 1) << 1)) * 16;
  if (w == 0) {
    f4 t[24];
#pragma unroll
    for (int n = 0; n < 16; ++n) t[n] = *(LAS f4*)(Ll + L_KVRED + ((n & 7) * 2 + (n >> 3)) * 1024);
#pragma unroll
    for (int n = 0; n < 8; ++n) t[16 + n] = *(LAS f4*)(L + L_ZP + n * 128 + (lane & 7) * 16);
    SB();
    const int so = WS_KV + (j * 16 + p) * KV_REC;
#pragma unroll
    for (int n = 0; n < 3; ++n) {
      const f4 a = ((t[8 * n] + t[8 * n + 1]) + (t[8 * n + 2] + t[8 * n + 3])) + ((t[8 * n + 4] + t[8 * n + 5]) + (t[8 * n + 6] + t[8 * n + 7]));
      if (n < 2 || lane < 8) st16(__builtin_bit_cast(v4u, a), rWS, lane * 16 + n * 1024, so);
    }
  } else {
    dma4(dWO, vO, w * (64 * 1024) + j * 64, 16 * 1024, lds0 + L_WO + w * 4096);
    if (w == 6) dma4(dWO, vO, j * 64, 16 * 1024, lds0 + L_WO);
  }
#define POISON_SLOTS() do { const v4u pz = {POISON, POISON, POISON, POISON}; \
    __builtin_amdgcn_raw_buffer_store_b128(pz, rWS, lane * 16, WS_O + ohoff, SC1); st16(pz, rWS, lane * 16, WS_H + ohoff); } while (0)
  if (w) POISON_SLOTS();
  const unsigned tag = epoch + 1u;
  GAS unsigned* const flags = (GAS unsigned*)(ws + WS_FLAG);
  f4 aq0 = {0, 0, 0, 0}, aq1 = aq0, cq4 = aq0;
  {
    v4u qf[3][2];
#pragma unroll
    for (int s = 0; s < 2; ++s) { qf[s][0] = trfrag(tN0 + L_WQ + s * 2048, 256); qf[s][1] = trfrag(tN1 + L_WQ + s * 2048, 256); }
#pragma unroll
    for (int s = 0; s < 16; ++s) {
      if (s + 2 < 16) { qf[(s + 2) % 3][0] = trfrag(tN0 + L_WQ + (s + 2) * 2048, 256); qf[(s + 2) % 3][1] = trfrag(tN1 + L_WQ + (s + 2) * 2048, 256); }
      aq0 = mfma32(qf[s % 3][0], xf[s], aq0);
      aq1 = mfma32(qf[s % 3][1], xf[s], aq1);
      if (s == QF && w == 0) {
        VMWAIT(0);
        if (lane == 0) __hip_atomic_store(flags + (p * 16 + j) * 32, tag, RLX_AGENT);
        STAMP(2);
        POISON_SLOTS();
      }
      SB();
    }
  }
  STAMP(5);
  const float mean1 = asf(__builtin_amdgcn_ds_bpermute((16 * q + i) * 4, asu(pp == 0 ? mr[0] : pp == 1 ? mr[1] : pp == 2 ? mr[2] : mr[3])));
  const float rstd1 = asf(__builtin_amdgcn_ds_bpermute((16 * q + i) * 4, asu(pp == 0 ? rs[0] : pp == 1 ? rs[1] : pp == 2 ? rs[2] : rs[3])));
  unsigned vW1[7];
#pragma unroll
  for (int n = 0; n < 7; ++n) { const unsigned c = n * 64 + lane, r = (c * 4682u) >> 16; vW1[n] = r * 3584 + (c - r * 14) * 16 - (n & 3) * 1024; }
  const int rd1 = (4 * g + q) * 224 + pp * 8;
  if (w == 0) {
    for (unsigned sp = 0; sp < (1u << 15); ++sp) {
      const unsigned v = lane < 16 ? __hip_atomic_load(flags + (i * 16 + j) * 32, RLX_AGENT) : tag;
      if (__all(v == tag)) break;
      COUNT(20);
    }
    VMWAIT(0);
  } else {
    unsigned bias = 0;
    if (w == 2 || w == 3) bias = __builtin_amdgcn_raw_buffer_load_b16(__builtin_amdgcn_make_buffer_rsrc((void*)(b1 + 112 * j), 0, 224, 0x00020000), (tid - 128) * 2, 0, 0);
    VMWAIT(0);
    if (w == 2 || w == 3) *(LAS float*)(L + L_B1 + (tid - 128) * 4) = bflo(bias);
    dma7(dW1, vW1, (5 + w) * (32 * 3584) + 224 * j, lds0 + L_W1 + (5 + w) * 7168);
    if (w == 7) dma7(dW1, vW1, 5 * (32 * 3584) + 224 * j, lds0 + L_W1 + 5 * 7168);
    if (w == 4 || w == 5) {
      LAS char* const b = (w & 1) ? tN1 : tN0;
#pragma unroll
      for (int h = 0; h < 4; ++h) {
        v4u t[4];
#pragma unroll
        for (int n = 0; n < 4; ++n) t[n] = trfrag(b + L_WQ + (h * 4 + n) * 2048, 256);
#pragma unroll
        for (int n = 0; n < 4; ++n) cq4 = mfma32(ones, t[n], cq4);
      }
      if (g == 0) *(LAS float*)(L + L_CQ + ((w & 1) * 16 + i) * 4) = cq4[0];
    }
  }
  asm volatile("" :: "v"(tch));
  BAR();
  if (tid == 0) __hip_atomic_store(flags + (512 + p * 16 + j) * 32, tag, RLX_AGENT);
  STAMP(6);
  v2u pq0, pq1;
  {
    const int so = WS_KV + (j * 16 + 4 * (w >> 1)) * KV_REC;
    v4u t[4];
#pragma unroll
    for (int n = 0; n < 4; ++n) t[n] = __builtin_amdgcn_raw_buffer_load_b128(rWS, lane * 16 + (w & 1) * 1024, so + n * KV_REC, SC1);
    const v4u zt = __builtin_amdgcn_raw_buffer_load_b128(rWS, q * KV_REC + 2048 + (w & 1) * 64 + pp * 16, so, SC1);
    SB();
    const f4 c0 = *(LAS f4*)(L + L_CQ + (4 * g) * 4), c1 = *(LAS f4*)(L + L_CQ + (16 + 4 * g) * 4);
    float a[4], b[4];
#pragma unroll
    for (int r = 0; r < 4; ++r) {
      a[r] = phi((aq0[r] - mean1 * c0[r]) * rstd1);
      b[r] = phi((aq1[r] - mean1 * c1[r]) * rstd1);
    }
    pq0 = v2u{pk(a[0], a[1]), pk(a[2], a[3])};
    pq1 = v2u{pk(b[0], b[1]), pk(b[2], b[3])};
    asm volatile("" : "+v"(pq0), "+v"(pq1));
    SB();
    const f4 s = (__builtin_bit_cast(f4, t[0]) + __builtin_bit_cast(f4, t[1])) + (__builtin_bit_cast(f4, t[2]) + __builtin_bit_cast(f4, t[3]));
    *(LAS f4*)(Ll + L_CONS + w * 1024) = s;
    f4 z = __builtin_bit_cast(f4, zt);
#pragma unroll
    for (int r = 0; r < 4; ++r) {
      z[r] += asf(__builtin_amdgcn_mov_dpp(asu(z[r]), 0x124, 0xf, 0xf, true));
      z[r] += asf(__builtin_amdgcn_mov_dpp(asu(z[r]), 0x128, 0xf, 0xf, true));
    }
    if (lane < 4) *(LAS f4*)(L + L_ZC + w * 64 + lane * 16) = z;
  }
  BAR();
  STAMP(7);
  {
    f4 k0 = {0, 0, 0, 0}, k1 = k0, z0 = k0, z1 = k0;
#pragma unroll
    for (int n = 0; n < 4; ++n) {
      k0 += *(LAS f4*)(Ll + L_CONS + (2 * n) * 1024);
      k1 += *(LAS f4*)(Ll + L_CONS + (2 * n + 1) * 1024);
      z0 += *(LAS f4*)(L + L_ZC + (2 * n) * 64 + g * 16);
      z1 += *(LAS f4*)(L + L_ZC + (2 * n + 1) * 64 + g * 16);
    }
    const f4 z4 = {0, 0, 0, 0};
    const f4 n0 = mfma16(v2u{pk(k0[0], k0[1]), pk(k0[2], k0[3])}, pq0, z4), n1 = mfma16(v2u{pk(k1[0], k1[1]), pk(k1[2], k1[3])}, pq1, z4);
    const f4 d0 = mfma16(v2u{pk(z0[0], z0[1]), pk(z0[2], z0[3])}, pq0, z4), d1 = mfma16(v2u{pk(z1[0], z1[1]), pk(z1[2], z1[3])}, pq1, z4);
    const float r0 = __builtin_amdgcn_rcpf(d0[0] + 1e-6f), r1 = __builtin_amdgcn_rcpf(d1[0] + 1e-6f);
    const v4u o = {nopoison(pk(n0[0] * r0, n0[1] * r0)), nopoison(pk(n0[2] * r0, n0[3] * r0)), nopoison(pk(n1[0] * r1, n1[1] * r1)), nopoison(pk(n1[2] * r1, n1[3] * r1))};
    st16(o, rWS, lane * 16, WS_O + ohoff);
  }
  if (w == 0) {
    for (unsigned sp = 0; sp < (1u << 15); ++sp) {
      const unsigned v = lane < 16 ? __hip_atomic_load(flags + (512 + p * 16 + i) * 32, RLX_AGENT) : tag;
      if (__all(v == tag)) break;
    }
    asm volatile("" ::: "memory");
  }
  const int grp2 = w < 5 ? w : w + 8;
  dma7(dW1, vW1, grp2 * (32 * 3584) + 224 * j, lds0 + L_W1 + grp2 * 7168);
  __builtin_amdgcn_s_barrier();
  STAMP(8);
#define LOAD1(s) f[s] = __builtin_amdgcn_raw_buffer_load_b128(rWS, lane * 16 + ((s) & 3) * 1024, so + ((s) >> 2) * 4096, SC1)
#define LOAD16() do { _Pragma("unroll") for (int s = 0; s < PD; ++s) { LOAD1(s); SB(); } } while (0)
  {
    f4 a0, a1;
    const int so = WS_O + (p * 8 + w) * 16384;
    v4u f[16];
    LAS char* const o0 = opq(L + L_WO + (4 * g + q) * 64 + pp * 8 + (g & 1) * 32);
    LAS char* const o1 = opq(L + L_WO + (4 * g + q) * 64 + pp * 8 + ((g & 1) ^ 1) * 32);
    LOAD16();
    repack224(L + L_W1 + (5 + w) * 7168, rd1, lane);
#pragma nounroll
    for (unsigned tries = 0; tries < 4096; ++tries) {
      a0 = f4{0, 0, 0, 0}; a1 = a0;
      v4u wf[3][2];
#pragma unroll
      for (int s = 0; s < 2; ++s) { wf[s][0] = trfrag(o0 + s * 2048, 1024); wf[s][1] = trfrag(o1 + s * 2048, 1024); }
#pragma unroll
      for (int s = 0; s < 16; ++s) {
        if (s + 2 < 16) { wf[(s + 2) % 3][0] = trfrag(o0 + (s + 2) * 2048, 1024); wf[(s + 2) % 3][1] = trfrag(o1 + (s + 2) * 2048, 1024); }
        a0 = mfma32(wf[s % 3][0], f[s], a0);
        a1 = mfma32(wf[s % 3][1], f[s], a1);
        if (s + PD < 16) LOAD1(s + PD < 16 ? s + PD : 0);
        SB();
      }
      if (__all(a0[0] == a0[0])) break;
      unsigned m = 0;
#pragma unroll
      for (int s = 0; s < 16; ++s) m = fragmax(m, f[s]);
      if (__all(m != POISON)) break;
      COUNT(21);
      __builtin_amdgcn_s_sleep(4);
      asm volatile("" ::: "memory");
      LOAD16();
    }
    const v4u h = {nopoison(pk(a0[0] + bflo(xr0.x), a0[1] + bfhi(xr0.x))), nopoison(pk(a0[2] + bflo(xr0.y), a0[3] + bfhi(xr0.y))),
                   nopoison(pk(a1[0] + bflo(xr1.x), a1[1] + bfhi(xr1.x))), nopoison(pk(a1[2] + bflo(xr1.y), a1[3] + bfhi(xr1.y)))};
    st16(h, rWS, lane * 16, WS_H + ohoff);
    if (bid == 0 && tid == 0) __builtin_amdgcn_raw_buffer_store_b32(tag, rWS, WS_EPOCH, 0, SC1);
  }
  STAMP(9);
  repack224(L + L_W1 + grp2 * 7168, rd1, lane);
  const int th = j >> 3, c4 = j & 7;
  v4u w2r[7][4];
  const int vW2 = (((lane >> 3) & 1) * 16 + g * 4) * 1024 + (lane & 7) * 16, sW2 = 224 * w * 1024 + c4 * 128;
#define W2LOAD(s) do { _Pragma("unroll") for (int n_ = 0; n_ < 4; ++n_) w2r[s][n_] = __builtin_amdgcn_raw_buffer_load_b128(rW2, vW2 + n_ * 1024, sW2 + (s) * 32768, 0); } while (0)
#pragma unroll
  for (int s = 0; s < W2B; ++s) W2LOAD(s);
  BAR();
  STAMP(10);
  v4u hres;
  {
    constexpr int NF = 5;
    v4u f[16];
    const int so = WS_H + (p * 8 + w) * 16384;
    LAS char* const wp0 = opq(Ll + L_W1 + (j & 1) * 1024);
    LAS char* const wp1 = opq(wp0 + 8 * 7168);
#define W1F(s, t) ldf(((s) < 8 ? wp0 : wp1) + ((s) & 7) * 7168 + (t) * 1024)
    f4 acc[7];
    LOAD16();
    if (w < 7) {
      const float c = colsum(Ll + L_W1 + w * 1024, 7168);
      if (g == 0) *(LAS float*)(L + L_C1 + (w * 16 + i) * 4) = c;
    }
#pragma nounroll
    for (unsigned tries = 0; tries < 4096; ++tries) {
#pragma unroll
      for (int t = 0; t < NF; ++t) acc[t] = f4{0, 0, 0, 0};
      sm = acc[0]; gr = acc[0];
      v4u wf[4];
#pragma unroll
      for (int n = 0; n < 3; ++n) wf[n] = W1F(n / NF, n % NF);
#pragma unroll
      for (int n = 0; n < 16 * NF; ++n) {
        if (n + 3 < 16 * NF) wf[(n + 3) & 3] = W1F((n + 3) / NF, (n + 3) % NF);
        acc[n % NF] = mfma32(wf[n & 3], f[n / NF], acc[n % NF]);
        const v4u a = f[n / NF];
        if (n % NF == 0) sm = mfma32(ones, a, sm);
        if (n % NF == 2) gr = mfma32(a, a, gr);
        if (n % NF == 1 && n / NF + PD < 16) LOAD1(n / NF + PD < 16 ? n / NF + PD : 0);
        SB();
      }
      asm volatile("" : "+v"(sm), "+v"(gr));
      if (__all(acc[0][0] == acc[0][0])) break;
      unsigned m = 0;
#pragma unroll
      for (int s = 0; s < 16; ++s) m = fragmax(m, f[s]);
      if (__all(m != POISON)) break;
      COUNT(22);
      __builtin_amdgcn_s_sleep(4);
      asm volatile("" ::: "memory");
      LOAD16();
    }
    const float sq = asf(__builtin_amdgcn_ds_bpermute((16 * q + i) * 4, asu(pp == 0 ? gr[0] : pp == 1 ? gr[1] : pp == 2 ? gr[2] : gr[3])));
    const float mean2 = sm[0] * (1.f / 512.f);
    float rstd2 = __builtin_amdgcn_rsqf(fmaxf(sq * (1.f / 512.f) - mean2 * mean2, 0.f) + 1e-5f);
    float nm = -mean2 * rstd2;
    asm volatile("" : "+v"(nm), "+v"(rstd2));
    STAMP(11);
    BAR();
    STAMP(12);
    const int aoff = WS_A + ((p * 8 + w) * 56 + ((7 * j + (j & 1)) >> 1)) * 1024;
    LAS char* const cp = L + L_C1 + ((j & 1) * 16 + 4 * g) * 4;
    LAS char* const cs = L + L_C1 + (((j & 1) ? 0 : 96) + 4 * g) * 4;
    LAS char* const ws0 = opq(Ll + L_W1 + ((j & 1) ? 0 : 6144));
    LAS char* const ws1 = opq(ws0 + 8 * 7168);
#define W1S(s) ldf(((s) < 8 ? ws0 : ws1) + ((s) & 7) * 7168)
    acc[5] = f4{0, 0, 0, 0}; acc[6] = acc[5];
    f4 cq[2], bq[2];
    v4u wf[2][4];
    cq[0] = *(LAS f4*)cp; bq[0] = *(LAS f4*)(cp + (L_B1 - L_C1));
    wf[0][0] = W1F(0, 5); wf[0][1] = W1S(0); wf[0][2] = W1F(1, 5); wf[0][3] = W1S(1);
    hres = __builtin_amdgcn_raw_buffer_load_b128(rWS, lane * 16, WS_H + ((p * 8 + 4 * th + (w >> 1)) * 16 + 2 * c4 + (w & 1)) * 1024, SC1);
    unsigned e[14];
#pragma unroll
    for (int k = 0; k < 14; ++k) {
      const int t = k >> 1, hf = k & 1;
      if (hf == 0 && t < 6) { LAS char* const cc = t + 1 < 6 ? cp + (t + 1) * 64 : cs; cq[(t + 1) & 1] = *(LAS f4*)cc; bq[(t + 1) & 1] = *(LAS f4*)(cc + (L_B1 - L_C1)); }
      if (k + 1 < 8) { wf[(k + 1) & 1][0] = W1F(2 * k + 2, 5); wf[(k + 1) & 1][1] = W1S(2 * k + 2); wf[(k + 1) & 1][2] = W1F(2 * k + 3, 5); wf[(k + 1) & 1][3] = W1S(2 * k + 3); }
      if (k < 4 * W2C) w2r[W2B + (k < 4 * W2C ? k : 0) / 4][k & 3] = __builtin_amdgcn_raw_buffer_load_b128(rW2, vW2 + (k & 3) * 1024, sW2 + (W2B + k / 4) * 32768, 0);
      if (k < 8) {
        acc[5] = mfma32(wf[k & 1][0], f[2 * k], acc[5]);
        acc[6] = mfma32(wf[k & 1][1], f[2 * k], acc[6]);
        acc[5] = mfma32(wf[k & 1][2], f[2 * k + 1], acc[5]);
        acc[6] = mfma32(wf[k & 1][3], f[2 * k + 1], acc[6]);
      }
      const float y0 = mish(acc[t][2 * hf] * rstd2 + (nm * cq[t & 1][2 * hf] + bq[t & 1][2 * hf]));
      const float y1 = mish(acc[t][2 * hf + 1] * rstd2 + (nm * cq[t & 1][2 * hf + 1] + bq[t & 1][2 * hf + 1]));
      e[k] = pk(y0, y1);
      if ((k & 3) == 3) st16(v4u{e[k - 3], e[k - 2], e[k - 1], e[k]}, rWS, lane * 16 + (k >> 2) * 1024, aoff);
      if (k == 13) __builtin_amdgcn_raw_buffer_store_b64(v2u{e[12], e[13]}, rWS, lane * 16 + (j & 1) * 8, aoff + ((j & 1) ? -1024 : 3072), SC1);
      SB();
    }
  }
  STAMP(13);
#pragma unroll
  for (int s = 0; s < W2B; ++s) { w2pack(w2r[s]); SB(); }
  VMWAIT(0);
  BAR();
  if (tid == 0) __hip_atomic_store(flags + (256 + p * 16 + j) * 32, tag, RLX_AGENT);
  STAMP(14);
#pragma unroll
  for (int s = W2B + W2C; s < 7; ++s) W2LOAD(s);
  SB();
  *(LAS v4u*)(Ll + L_HRES + w * 1024) = hres;
#pragma unroll
  for (int s = W2B; s < W2B + W2C; ++s) { w2pack(w2r[s]); SB(); }
  {
    GAS unsigned* const fp = flags + (256 + p * 16 + 2 * w + (lane & 1)) * 32;
    for (unsigned sp = 0; sp < (1u << 15); ++sp) {
      const unsigned v = lane < 2 ? __hip_atomic_load(fp, RLX_AGENT) : tag;
      if (__all(v == tag)) break;
      COUNT(23);
    }
    asm volatile("" ::: "memory");
  }
  STAMP(15);
  {
    const int so = WS_A + ((p * 8 + 4 * th) * 56 + 7 * w) * 1024;
    constexpr int NB = PD + 1;
    v4u f[NB];
#define ALOAD(n) __builtin_amdgcn_raw_buffer_load_b128(rWS, lane * 16 + (((n) % 7) & 3) * 1024, so + (((n) / 7) * 56 + (((n) % 7) & 4)) * 1024, SC1)
#pragma unroll
    for (int n = 0; n < NB; ++n) { f[n] = ALOAD(n); SB(); }
#pragma unroll
    for (int s = W2B + W2C; s < 7; ++s) { w2pack(w2r[s]); SB(); }
    LAS char* const wr = L + (w * 1024 + 16 * g + (i ^ (4 * g))) * 16;
    f4 acc[4];
#pragma unroll
    for (int n = 0; n < 28; ++n) {
      const int s = n % 7, t = n / 7;
#pragma unroll
      for (int u = 0; u < 4; ++u) acc[u] = mfma32(w2r[s][u], f[n % NB], s ? acc[u] : f4{0, 0, 0, 0});
      if (n + NB < 28) f[n % NB] = ALOAD(n + NB < 28 ? n + NB : 0);
      if (s == 6) {
#pragma unroll
        for (int u = 0; u < 4; ++u) *(LAS f4*)(wr + (t * 4 + u) * 1024) = acc[u];
      }
      SB();
    }
    STAMP(16);
    BAR();
    STAMP(17);
    int tid2 = w * 64 + lane;
    asm volatile("" : "+v"(tid2));
    const int tok = tid2 >> 3, pc = tid2 & 7, gg = (pc >> 2) + 2 * (pc & 1);
    LAS char* rp = L + (((tok >> 4) * 4) * 64 + 16 * gg + ((tok & 15) ^ (4 * gg))) * 16 + ((pc >> 1) & 1) * 8;
    LAS char* const rq = opq(rp + 65536);
    LAS char* const hp = L + L_HRES + ((tok >> 4) * 2 + (pc >> 2)) * 1024 + (16 * (pc & 1) + (tok & 15)) * 16 + ((pc >> 1) & 1) * 8;
    const v2u hr0 = *(volatile LAS v2u*)hp, hr1 = *(volatile LAS v2u*)(hp + 512);
    f2 r[4];
#pragma unroll
    for (int hh = 0; hh < 2; ++hh) {
      f2 t[16];
#pragma unroll
      for (int n = 0; n < 16; ++n) t[n] = *(volatile LAS f2*)((hh ? rq : rp) + (n >> 2) * 16384 + (n & 3) * 1024);
#pragma unroll
      for (int u = 0; u < 4; ++u) {
        const f2 s = (t[u] + t[4 + u]) + (t[8 + u] + t[12 + u]);
        r[u] = hh ? r[u] + s : s;
      }
    }
    const v2u o0 = {pk(r[0][0] + bflo(hr0.x), r[1][0] + bfhi(hr0.x)), pk(r[2][0] + bflo(hr0.y), r[3][0] + bfhi(hr0.y))};
    const v2u o1 = {pk(r[0][1] + bflo(hr1.x), r[1][1] + bfhi(hr1.x)), pk(r[2][1] + bflo(hr1.y), r[3][1] + bfhi(hr1.y))};
    u16* op = out + (size_t)(p * 128 + th * 64 + tok) * 512 + 64 * c4 + 32 * (pc >> 2) + 16 * ((pc >> 1) & 1) + 4 * (pc & 1);
    *(v2u*)op = o0;
    *(v2u*)(op + 8) = o1;
  }
  STAMP(18);
  STAMP_FLUSH
}

extern "C" void kernel_launch(void* const* d_in, const int* in_sizes, int n_in, void* d_out, int out_size, void* d_ws, size_t ws_size,
                              hipStream_t stream) {
  (void)in_sizes; (void)n_in; (void)out_size; (void)ws_size;
  (void)hipFuncSetAttribute((const void*)fused_block, hipFuncAttributeMaxDynamicSharedMemorySize, LDS_BYTES);
  fused_block<<<256, 512, LDS_BYTES, stream>>>((const u16*)d_in[0], (const u16*)d_in[1], (const u16*)d_in[2], (const u16*)d_in[4],
                                               (const u16*)d_in[5], (const u16*)d_in[6], (u16*)d_out, (char*)d_ws);
}
```

```cpp
#include <hip/hip_runtime.h>
#include <hip/hip_bf16.h>

typedef unsigned short u16;
typedef unsigned v4u __attribute__((ext_vector_type(4)));
typedef unsigned v2u __attribute__((ext_vector_type(2)));
typedef float f4 __attribute__((ext_vector_type(4)));
typedef float f2 __attribute__((ext_vector_type(2)));
typedef short s4 __attribute__((ext_vector_type(4)));
typedef __bf16 b8 __attribute__((ext_vector_type(8)));
typedef __bf16 b2 __attribute__((ext_vector_type(2)));
#define GAS __attribute__((address_space(1)))
#define LAS __attribute__((address_space(3)))
#define DI __device__ __forceinline__
#define RLX_AGENT __ATOMIC_RELAXED, __HIP_MEMORY_SCOPE_AGENT

#define MAPMODE 0
#define TOUCH 0
#define TOUCH2 1
#define QF 5
#define TOUCH3 0
#define ROT 1
#define PD 6
#define W2B 2
#define W2C 3

constexpr int CODE_KB = 24;
constexpr int WS_EPOCH = 0;
constexpr int WS_FLAG = 4096;
constexpr int WS_KV = 131072;
constexpr int KV_REC = 2304;
constexpr int WS_O = 1 << 20;
constexpr int WS_H = 3 << 20;
constexpr int WS_A = 5 << 20;
constexpr int WS_BYTES = 12 << 20;
#define STAMP(n)
#define STAMPX(n)
#define STAMP_DECL
#define STAMP_FLUSH
#define COUNT(n)

constexpr int L_WQ = 0, L_WK = 32768, L_WV = 65536, L_KVRED = 98304, L_WO = 114688, L_SCR = 147456;
constexpr int L_W1 = 0;
constexpr int L_CONS = L_SCR;
constexpr int L_ZP = L_SCR + 8192;
constexpr int L_ZC = L_ZP + 1024;
constexpr int L_CKV = L_ZC + 512;
constexpr int L_CQ = L_CKV + 2048;
constexpr int L_C1 = L_CQ + 128;
constexpr int L_B1 = L_C1 + 512;
constexpr int L_HRES = 131072;
constexpr int LDS_BYTES = 163840;

constexpr unsigned POISON = 0xFFFFFFFFu;
constexpr unsigned ONE2 = 0x3F803F80u;
constexpr int SC1 = 16;
constexpr float LOG2E = 1.4426950408889634f;

DI float asf(unsigned u) { return __builtin_bit_cast(float, u); }
DI unsigned asu(float f) { return __builtin_bit_cast(unsigned, f); }
DI f4 mfma32(v4u a, v4u b, f4 c) { return __builtin_amdgcn_mfma_f32_16x16x32_bf16(__builtin_bit_cast(b8, a), __builtin_bit_cast(b8, b), c, 0, 0, 0); }
DI f4 mfma16(v2u a, v2u b, f4 c) { return __builtin_amdgcn_mfma_f32_16x16x16bf16_1k(__builtin_bit_cast(s4, a), __builtin_bit_cast(s4, b), c, 0, 0, 0); }
DI unsigned pk(float lo, float hi) { f2 v = {lo, hi}; return __builtin_bit_cast(unsigned, __builtin_convertvector(v, b2)); }
DI float bflo(unsigned u) { return asf(u << 16); }
DI float bfhi(unsigned u) { return asf(u & 0xffff0000u); }
DI unsigned nopoison(unsigned u) { return u < 0xFFFFFFFEu ? u : 0xFFFFFFFEu; }
DI v2u trd(LAS char* p) { return __builtin_bit_cast(v2u, __builtin_amdgcn_ds_read_tr16_b64_v4i16((LAS s4*)p)); }
DI v4u trfrag(LAS char* p, int second) { v2u a = trd(p), b = trd(p + second); return v4u{a.x, a.y, b.x, b.y}; }
DI v4u ldf(LAS char* p) { return *(LAS v4u*)p; }
DI float dot2(unsigned a, unsigned b, float c) { return __builtin_amdgcn_fdot2_f32_bf16(__builtin_bit_cast(b2, a), __builtin_bit_cast(b2, b), c, false); }
DI float rowsum4(float v) {
  auto r = __builtin_amdgcn_permlane32_swap(asu(v), asu(v), false, false);
  const float s = asf(r[0]) + asf(r[1]);
  auto t = __builtin_amdgcn_permlane16_swap(asu(s), asu(s), false, false);
  return asf(t[0]) + asf(t[1]);
}
DI v4u mksrd(const void* p, unsigned bytes) {
  unsigned long long a = (unsigned long long)p;
  return v4u{(unsigned)a, (unsigned)(a >> 32) & 0xffffu, bytes, 0x00020000u};
}
DI void dma16(v4u srd, unsigned voff, unsigned soff, unsigned lds) {
  asm volatile("s_mov_b32 m0, %3\n\ts_nop 0\n\tbuffer_load_dwordx4 %0, %1, %2 offen lds" ::"v"(voff), "s"(srd), "s"(soff), "s"(lds) : "memory");
}
DI void dma4(v4u srd, unsigned voff, unsigned s0, int step, unsigned lds) {
  asm volatile("s_mov_b32 m0, %6\n\ts_nop 0\n\tbuffer_load_dwordx4 %0, %1, %2 offen lds\n\tbuffer_load_dwordx4 %0, %1, %3 offen offset:1024 lds\n\t"
               "buffer_load_dwordx4 %0, %1, %4 offen offset:2048 lds\n\tbuffer_load_dwordx4 %0, %1, %5 offen offset:3072 lds"
               ::"v"(voff), "s"(srd), "s"(s0), "s"(s0 + step - 1024), "s"(s0 + 2 * step - 2048), "s"(s0 + 3 * step - 3072), "s"(lds) : "memory");
}
DI void dma2(v4u srd, unsigned voff, unsigned s0, int step, unsigned lds) {
  asm volatile("s_mov_b32 m0, %4\n\ts_nop 0\n\tbuffer_load_dwordx4 %0, %1, %2 offen lds\n\tbuffer_load_dwordx4 %0, %1, %3 offen offset:1024 lds"
               ::"v"(voff), "s"(srd), "s"(s0), "s"(s0 + step - 1024), "s"(lds) : "memory");
}
DI void dma7(v4u srd, const unsigned (&v)[7], unsigned so, unsigned lds) {
  asm volatile("s_mov_b32 m0, %9\n\ts_nop 0\n\tbuffer_load_dwordx4 %0, %7, %8 offen lds\n\tbuffer_load_dwordx4 %1, %7, %8 offen offset:1024 lds\n\t"
               "buffer_load_dwordx4 %2, %7, %8 offen offset:2048 lds\n\tbuffer_load_dwordx4 %3, %7, %8 offen offset:3072 lds\n\t"
               "s_mov_b32 m0, %10\n\ts_nop 0\n\tbuffer_load_dwordx4 %4, %7, %8 offen lds\n\tbuffer_load_dwordx4 %5, %7, %8 offen offset:1024 lds\n\t"
               "buffer_load_dwordx4 %6, %7, %8 offen offset:2048 lds"
               ::"v"(v[0]), "v"(v[1]), "v"(v[2]), "v"(v[3]), "v"(v[4]), "v"(v[5]), "v"(v[6]), "s"(srd), "s"(so), "s"(lds), "s"(lds + 4096) : "memory");
}
DI void st16(v4u v, __amdgpu_buffer_rsrc_t r, int voff, int soff) {
  __builtin_amdgcn_raw_buffer_store_b128(v, r, voff, soff, SC1);
  asm volatile("s_nop 1" ::"v"(v) : "memory");
}
DI LAS char* opq(LAS char* p) { asm volatile("" : "+v"(p)); return p; }
#define LGKM0() asm volatile("s_waitcnt lgkmcnt(0)" ::: "memory")
#define BAR() do { LGKM0(); __builtin_amdgcn_s_barrier(); asm volatile("" ::: "memory"); } while (0)
#define VMWAIT(n) asm volatile("s_waitcnt vmcnt(%0)" ::"i"(n) : "memory")
#define SB() __builtin_amdgcn_sched_barrier(0)
DI unsigned umax3(unsigned a, unsigned b, unsigned c) { return max(max(a, b), c); }
DI unsigned fragmax(unsigned m, v4u f) { return umax3(umax3(m, f.x, f.y), f.z, f.w); }

template <int NB> DI void repack64(LAS char* blk, int rdoff, int second, int lane) {
  v4u t[2 * NB];
#pragma unroll
  for (int n = 0; n < NB; ++n) { t[2 * n] = trfrag(blk + n * 2048 + rdoff, second); t[2 * n + 1] = trfrag(blk + n * 2048 + rdoff + 32, second); }
  LGKM0();
#pragma unroll
  for (int n = 0; n < 2 * NB; ++n) *(LAS v4u*)(blk + n * 1024 + lane * 16) = t[n];
}
DI void repack224(LAS char* blk, int rdoff, int lane) {
  v4u t[7];
#pragma unroll
  for (int n = 0; n < 7; ++n) t[n] = trfrag(blk + rdoff + n * 32, 16 * 224);
  LGKM0();
#pragma unroll
  for (int n = 0; n < 7; ++n) *(LAS v4u*)(blk + n * 1024 + lane * 16) = t[n];
}
DI float colsum(LAS char* p, int step) {
  const v4u ones = {ONE2, ONE2, ONE2, ONE2};
  f4 c = {0.f, 0.f, 0.f, 0.f};
  v4u t[16];
#pragma unroll
  for (int h = 0; h < 2; ++h) {
    LAS char* const b = opq(p + h * 8 * step);
#pragma unroll
    for (int s = 0; s < 8; ++s) t[h * 8 + s] = ldf(b + s * step);
  }
  SB();
#pragma unroll
  for (int s = 0; s < 16; ++s) c = mfma32(ones, t[s], c);
  return c[0];
}
DI float phi(float t) { return fmaxf(t, 0.f) + __builtin_amdgcn_exp2f(fminf(t, 0.f) * LOG2E) + 1e-6f; }
DI float mish(float t) {
  const float n = __builtin_amdgcn_exp2f(fminf(t, 30.f) * LOG2E);
  const float u = n * (n + 2.f);
  return t * u * __builtin_amdgcn_rcpf(u + 2.f);
}
#define STATS(a) do { s0 = dot2(a.x, ONE2, s0); s1 = dot2(a.y, ONE2, s1); s0 = dot2(a.z, ONE2, s0); s1 = dot2(a.w, ONE2, s1); \
                      ss0 = dot2(a.x, a.x, ss0); ss1 = dot2(a.y, a.y, ss1); ss0 = dot2(a.z, a.z, ss0); ss1 = dot2(a.w, a.w, ss1); } while (0)

DI void w2pack(v4u (&r)[4]) {
  v2u lo[4], hi[4];
#pragma unroll
  for (int n = 0; n < 4; ++n)
#pragma unroll
    for (int d = 0; d < 2; ++d) {
      const unsigned a = r[n][d], b = r[n][2 + d];
      lo[n][d] = __builtin_amdgcn_update_dpp(a, b, 0x128, 0xf, 0xc, false);
      hi[n][d] = __builtin_amdgcn_update_dpp(b, a, 0x128, 0xf, 0x3, false);
    }
#pragma unroll
  for (int u = 0; u < 4; ++u) {
    const unsigned sel = (u & 1) ? 0x07060302u : 0x05040100u;
    r[u] = v4u{__builtin_amdgcn_perm(lo[1][u >> 1], lo[0][u >> 1], sel), __builtin_amdgcn_perm(lo[3][u >> 1], lo[2][u >> 1], sel),
               __builtin_amdgcn_perm(hi[1][u >> 1], hi[0][u >> 1], sel), __builtin_amdgcn_perm(hi[3][u >> 1], hi[2][u >> 1], sel)};
  }
}

__global__ void __launch_bounds__(512, 2)
fused_block(const u16* __restrict__ x, const u16* __restrict__ wqkv, const u16* __restrict__ wo, const u16* __restrict__ w1,
            const u16* __restrict__ b1, const u16* __restrict__ w2, u16* __restrict__ out, char* ws) {
  extern __shared__ __attribute__((aligned(16))) char smem_[];
  LAS char* L = (LAS char*)smem_;
  const int tid = threadIdx.x, lane = tid & 63, w = __builtin_amdgcn_readfirstlane(tid >> 6);
  const int g = lane >> 4, i = lane & 15, q = (lane >> 2) & 3, pp = lane & 3;
  const int bid = blockIdx.x, xcd = bid & 7, rk = bid >> 3;
  const int p = 2 * xcd + (rk >> 4), j = rk & 15;
  const int rot = ROT ? 2 * (j & 1) + 4 * (p & 1) + (j >> 1) : 0;
  STAMP_DECL
  STAMP(0);
  const v4u dQKV = mksrd(wqkv, 512 * 1536 * 2), dWO = mksrd(wo, 512 * 512 * 2), dW1 = mksrd(w1, 512 * 1792 * 2);
  const v4u dX = mksrd((const char*)x - 4096, 2048 * 512 * 2 + 4096);
  const __amdgpu_buffer_rsrc_t rWS = __builtin_amdgcn_make_buffer_rsrc(ws, 0, WS_BYTES, 0x00020000);
  const __amdgpu_buffer_rsrc_t rW2 = __builtin_amdgcn_make_buffer_rsrc((void*)w2, 0, 1792 * 512 * 2, 0x00020000);
  const unsigned lds0 = (unsigned)(size_t)L;
  LAS char* const Ll = L + lane * 16;

  unsigned tch = 0;
  const unsigned sw8 = (pp ^ ((lane >> 5) << 1)) * 16;
  const unsigned vW = (lane >> 2) * 3072 + sw8;
  const unsigned vX0 = (lane >> 3) * 1024 + ((lane & 7) ^ g) * 16, vX1 = (vX0 ^ 64) + 8192;
  const unsigned sX = (p * 128 + w * 16) * 1024 + 4096;
  const unsigned sW = (w & 3) * (16 * 3072) + (1 + (w >> 2)) * 1024 + j * 64, lW = lds0 + L_WK + (w >> 2) * 32768 + (w & 3) * 1024;
#define XSLOT(c) ((c) % 3 == 0 ? L_KVRED + w * 2048 : L_WO + w * 4096 + ((c) % 3 - 1) * 2048)
#define WLOAD(c) dma16(dQKV, vW, sW + (((c) + rot) & 7) * (64 * 3072), lW + (c) * 4096)
#define XLOAD1(c, h) dma16(dX, (h) ? vX1 : vX0, sX + (((c) + rot) & 7) * 128, lds0 + XSLOT(c) + (h) * 1024)
#define XLOAD(c) do { XLOAD1(c, 0); XLOAD1(c, 1); } while (0)
#define QLOAD1(n) dma16(dQKV, vW, (((w + rot) & 7) * 64 + (n) * 16) * 3072 + j * 64, lds0 + L_WQ + w * 4096 + (n) * 1024)
  unsigned epoch;
  asm volatile("global_load_dword %0, %1, %2 sc1" : "=v"(epoch) : "v"(0), "s"(ws) : "memory");
  WLOAD(0); XLOAD(0);
  __builtin_amdgcn_s_barrier();
  WLOAD(1); XLOAD(1);
  STAMP(1);
  LAS char* const tN0 = L + (8 * g + q) * 64 + pp * 8 + (g & 1) * 32;
  LAS char* const tN1 = L + (8 * g + q) * 64 + pp * 8 + ((g & 1) ^ 1) * 32;
  LAS char* const kN0 = opq(tN0 + L_WK);
  LAS char* const kN1 = opq(tN1 + L_WK);
  LAS char* const kC = opq(((w & 1) ? tN1 : tN0) + L_WK + ((w >> 1) & 1) * 32768 + (w >> 2) * 2048);
#define WFRAG(s, t) trfrag((((t) & 1) ? kN1 : kN0) + ((t) >> 1) * 32768 + (s) * 2048, 256)
  v4u ones = {ONE2, ONE2, ONE2, ONE2};
  asm volatile("" : "+v"(ones));
  v4u xf[16];
  v2u xr0, xr1;
  f4 ak0 = {0, 0, 0, 0}, ak1 = ak0, av0 = ak0, av1 = ak0, sm = ak0, gr = ak0, cs = ak0;
  const int xrd = i * 128 + (g ^ (i >> 1)) * 16;
  LAS char* const xa0 = L + L_KVRED + w * 2048 + xrd;
  LAS char* const xa1 = L + L_KVRED + w * 2048 + (xrd ^ 64);
  LAS char* const xb0 = L + L_WO + w * 4096 + xrd;
  LAS char* const xb1 = L + L_WO + w * 4096 + (xrd ^ 64);
#define XREAD(c) do { xf[2 * (c)] = ldf((c) % 3 ? xb0 + ((c) % 3 - 1) * 2048 : xa0); xf[2 * (c) + 1] = ldf((c) % 3 ? xb1 + ((c) % 3 - 1) * 2048 : xa1); } while (0)
  v4u wf[2][8], cf;
  f2 cp2[4];
  const int cres = ((j >> 1) - rot) & 7;
#pragma unroll
  for (int c = -1; c < 8; ++c) {
    if (c >= 0) {
      sm = mfma32(xf[2 * c], ones, sm);
      gr = mfma32(xf[2 * c], xf[2 * c], gr);
      SB();
    }
    if (c < 7) VMWAIT(c < 6 ? 3 : 2); else LGKM0();
    __builtin_amdgcn_s_barrier();
    asm volatile("" ::: "memory");
    STAMPX(11 + c);
    if (c < 7) { XREAD(c + 1); cf = trfrag(kC + (c + 1) * 4096, 256); }
    else {
#pragma unroll
      for (int t = 0; t < 4; ++t) cp2[t] = *(LAS f2*)(L + L_CKV + (t * 16 + i) * 8);
    }
    SB();
#pragma unroll
    for (int h = 0; h < 2; ++h) {
      if (c >= 0 && h == 1) {
        sm = mfma32(xf[2 * c + 1], ones, sm);
        gr = mfma32(xf[2 * c + 1], xf[2 * c + 1], gr);
      }
      if (h == 1) { if (c < 5) XLOAD1(c + 3, 0); else if (c < 7) QLOAD1(2 * c - 10); }
      SB();
      if (c >= 0) {
        ak0 = mfma32(xf[2 * c + h], wf[c & 1][4 * h], ak0);
        ak1 = mfma32(xf[2 * c + h], wf[c & 1][4 * h + 1], ak1);
      }
      if (c < 7) { wf[(c + 1) & 1][4 * h] = WFRAG(2 * c + 2 + h, 0); wf[(c + 1) & 1][4 * h + 1] = WFRAG(2 * c + 2 + h, 1); }
      if (h == 0) { if (c < 5) WLOAD(c + 3); }
      SB();
      if (c >= 0) {
        av0 = mfma32(xf[2 * c + h], wf[c & 1][4 * h + 2], av0);
        av1 = mfma32(xf[2 * c + h], wf[c & 1][4 * h + 3], av1);
      }
      if (c < 7) { wf[(c + 1) & 1][4 * h + 2] = WFRAG(2 * c + 2 + h, 2); wf[(c + 1) & 1][4 * h + 3] = WFRAG(2 * c + 2 + h, 3); }
      if (h == 1) { if (c < 5) XLOAD1(c + 3, 1); else if (c < 7) QLOAD1(2 * c - 9); }
      SB();
    }
    if (c < 7) {
      cs = mfma32(ones, cf, cs);
      if (c + 1 == cres) {
        LAS char* const rp = L + XSLOT(c + 1) + i * 128 + (g & 1) * 8;
        xr0 = *(LAS v2u*)(rp + (((j & 1) * 4 + (g >> 1)) ^ (i >> 1)) * 16);
        xr1 = *(LAS v2u*)(rp + (((j & 1) * 4 + (g >> 1) + 2) ^ (i >> 1)) * 16);
        LGKM0();
      }
    }
    if (c == 6) {
      const unsigned long long ta = w == 1 ? (unsigned long long)wo + bid * 2048 : w == 2 ? (unsigned long long)w1 + bid * 7168 : w == 3 ? (unsigned long long)w2 + bid * 7168
                                           : (unsigned long long)ws + WS_FLAG + (bid + (w & 3) * 256) * 128;
      const int nl = w == 0 ? 0 : w == 1 ? 16 : w < 4 ? 56 : w < 7 ? 1 : 0;
      if (lane < nl) asm volatile("global_load_dword %0, %1, %2" : "+v"(tch) : "v"(lane * 128), "s"(ta) : "memory");
      SB();
    }
    if (c == 6 && g == 0) *(LAS float*)(L + L_CKV + ((w & 3) * 16 + i) * 8 + (w >> 2) * 4) = cs[0];
  }
  STAMP(3);
  float mr[4], rs[4];
  {
    float cc[4];
#pragma unroll
    for (int t = 0; t < 4; ++t) cc[t] = cp2[t][0] + cp2[t][1];
#pragma unroll
    for (int r = 0; r < 4; ++r) {
      const float gd = gr[r];
      const float sq = asf(__builtin_amdgcn_ds_bpermute((20 * g + r) * 4, asu(gd)));
      mr[r] = sm[r] * (1.f / 512.f);
      rs[r] = __builtin_amdgcn_rsqf(fmaxf(sq * (1.f / 512.f) - mr[r] * mr[r], 0.f) + 1e-5f);
    }
    float k0[4], k1[4], v0[4], v1[4];
#pragma unroll
    for (int r = 0; r < 4; ++r) {
      k0[r] = phi((ak0[r] - mr[r] * cc[0]) * rs[r]);
      k1[r] = phi((ak1[r] - mr[r] * cc[1]) * rs[r]);
      v0[r] = (av0[r] - mr[r] * cc[2]) * rs[r];
      v1[r] = (av1[r] - mr[r] * cc[3]) * rs[r];
    }
    const v2u pk0 = {pk(k0[0], k0[1]), pk(k0[2], k0[3])}, pk1 = {pk(k1[0], k1[1]), pk(k1[2], k1[3])};
    const v2u pv0 = {pk(v0[0], v0[1]), pk(v0[2], v0[3])}, pv1 = {pk(v1[0], v1[1]), pk(v1[2], v1[3])};
    const v2u one2 = {ONE2, ONE2};
    const f4 z4 = {0, 0, 0, 0};
    const f4 kv0 = mfma16(pk0, pv0, z4), kv1 = mfma16(pk1, pv1, z4), z0 = mfma16(pk0, one2, z4), z1 = mfma16(pk1, one2, z4);
    *(LAS f4*)(Ll + L_KVRED + (w * 2 + 0) * 1024) = kv0;
    *(LAS f4*)(Ll + L_KVRED + (w * 2 + 1) * 1024) = kv1;
    if (i == 0) { *(LAS f4*)(L + L_ZP + w * 128 + g * 16) = z0; *(LAS f4*)(L + L_ZP + w * 128 + 64 + g * 16) = z1; }
  }
  if (TOUCH2 && w && w < 7) VMWAIT(1); else VMWAIT(0);
  asm volatile("" : "+v"(epoch));
  BAR();
  STAMP(4);
  const int ohoff = ((p * 8 + w) * 16 + j) * 1024;
  const unsigned vO = (lane >> 2) * 1024 + (pp ^ ((g & 1) << 1)) * 16;
  if (w == 0) {
    f4 t[24];
#pragma unroll
    for (int n = 0; n < 16; ++n) t[n] = *(LAS f4*)(Ll + L_KVRED + ((n & 7) * 2 + (n >> 3)) * 1024);
#pragma unroll
    for (int n = 0; n < 8; ++n) t[16 + n] = *(LAS f4*)(L + L_ZP + n * 128 + (lane & 7) * 16);
    SB();
    const int so = WS_KV + (j * 16 + p) * KV_REC;
#pragma unroll
    for (int n = 0; n < 3; ++n) {
      const f4 a = ((t[8 * n] + t[8 * n + 1]) + (t[8 * n + 2] + t[8 * n + 3])) + ((t[8 * n + 4] + t[8 * n + 5]) + (t[8 * n + 6] + t[8 * n + 7]));
      if (n < 2 || lane < 8) st16(__builtin_bit_cast(v4u, a), rWS, lane * 16 + n * 1024, so);
    }
  } else {
    dma4(dWO, vO, w * (64 * 1024) + j * 64, 16 * 1024, lds0 + L_WO + w * 4096);
    if (w == 6) dma4(dWO, vO, j * 64, 16 * 1024, lds0 + L_WO);
  }
#define POISON_SLOTS() do { const v4u pz = {POISON, POISON, POISON, POISON}; \
    __builtin_amdgcn_raw_buffer_store_b128(pz, rWS, lane * 16, WS_O + ohoff, SC1); st16(pz, rWS, lane * 16, WS_H + ohoff); } while (0)
  if (w) POISON_SLOTS();
  const unsigned tag = epoch + 1u;
  GAS unsigned* const flags = (GAS unsigned*)(ws + WS_FLAG);
  f4 aq0 = {0, 0, 0, 0}, aq1 = aq0, cq4 = aq0;
  {
    v4u qf[3][2];
#pragma unroll
    for (int s = 0; s < 2; ++s) { qf[s][0] = trfrag(tN0 + L_WQ + s * 2048, 256); qf[s][1] = trfrag(tN1 + L_WQ + s * 2048, 256); }
#pragma unroll
    for (int s = 0; s < 16; ++s) {
      if (s + 2 < 16) { qf[(s + 2) % 3][0] = trfrag(tN0 + L_WQ + (s + 2) * 2048, 256); qf[(s + 2) % 3][1] = trfrag(tN1 + L_WQ + (s + 2) * 2048, 256); }
      aq0 = mfma32(qf[s % 3][0], xf[s], aq0);
      aq1 = mfma32(qf[s % 3][1], xf[s], aq1);
      if (s == QF && w == 0) {
        VMWAIT(0);
        if (lane == 0) __hip_atomic_store(flags + (p * 16 + j) * 32, tag, RLX_AGENT);
        STAMP(2);
        POISON_SLOTS();
      }
      SB();
    }
  }
  STAMP(5);
  const float mean1 = asf(__builtin_amdgcn_ds_bpermute((16 * q + i) * 4, asu(pp == 0 ? mr[0] : pp == 1 ? mr[1] : pp == 2 ? mr[2] : mr[3])));
  const float rstd1 = asf(__builtin_amdgcn_ds_bpermute((16 * q + i) * 4, asu(pp == 0 ? rs[0] : pp == 1 ? rs[1] : pp == 2 ? rs[2] : rs[3])));
  unsigned vW1[7];
#pragma unroll
  for (int n = 0; n < 7; ++n) { const unsigned c = n * 64 + lane, r = (c * 4682u) >> 16; vW1[n] = r * 3584 + (c - r * 14) * 16 - (n & 3) * 1024; }
  const int rd1 = (4 * g + q) * 224 + pp * 8;
  if (w == 0) {
    for (unsigned sp = 0; sp < (1u << 15); ++sp) {
      const unsigned v = lane < 16 ? __hip_atomic_load(flags + (i * 16 + j) * 32, RLX_AGENT) : tag;
      if (__all(v == tag)) break;
      COUNT(20);
    }
    VMWAIT(0);
  } else {
    unsigned bias = 0;
    if (w == 2 || w == 3) bias = __builtin_amdgcn_raw_buffer_load_b16(__builtin_amdgcn_make_buffer_rsrc((void*)(b1 + 112 * j), 0, 224, 0x00020000), (tid - 128) * 2, 0, 0);
    VMWAIT(0);
    if (w == 2 || w == 3) *(LAS float*)(L + L_B1 + (tid - 128) * 4) = bflo(bias);
    dma7(dW1, vW1, (5 + w) * (32 * 3584) + 224 * j, lds0 + L_W1 + (5 + w) * 7168);
    if (w == 7) dma7(dW1, vW1, 5 * (32 * 3584) + 224 * j, lds0 + L_W1 + 5 * 7168);
    if (w == 4 || w == 5) {
      LAS char* const b = (w & 1) ? tN1 : tN0;
#pragma unroll
      for (int h = 0; h < 4; ++h) {
        v4u t[4];
#pragma unroll
        for (int n = 0; n < 4; ++n) t[n] = trfrag(b + L_WQ + (h * 4 + n) * 2048, 256);
#pragma unroll
        for (int n = 0; n < 4; ++n) cq4 = mfma32(ones, t[n], cq4);
      }
      if (g == 0) *(LAS float*)(L + L_CQ + ((w & 1) * 16 + i) * 4) = cq4[0];
    }
  }
  asm volatile("" :: "v"(tch));
  BAR();
  if (tid == 0) __hip_atomic_store(flags + (512 + p * 16 + j) * 32, tag, RLX_AGENT);
  STAMP(6);
  v2u pq0, pq1;
  {
    const int so = WS_KV + (j * 16 + 4 * (w >> 1)) * KV_REC;
    v4u t[4];
#pragma unroll
    for (int n = 0; n < 4; ++n) t[n] = __builtin_amdgcn_raw_buffer_load_b128(rWS, lane * 16 + (w & 1) * 1024, so + n * KV_REC, SC1);
    const v4u zt = __builtin_amdgcn_raw_buffer_load_b128(rWS, q * KV_REC + 2048 + (w & 1) * 64 + pp * 16, so, SC1);
    SB();
    const f4 c0 = *(LAS f4*)(L + L_CQ + (4 * g) * 4), c1 = *(LAS f4*)(L + L_CQ + (16 + 4 * g) * 4);
    float a[4], b[4];
#pragma unroll
    for (int r = 0; r < 4; ++r) {
      a[r] = phi((aq0[r] - mean1 * c0[r]) * rstd1);
      b[r] = phi((aq1[r] - mean1 * c1[r]) * rstd1);
    }
    pq0 = v2u{pk(a[0], a[1]), pk(a[2], a[3])};
    pq1 = v2u{pk(b[0], b[1]), pk(b[2], b[3])};
    asm volatile("" : "+v"(pq0), "+v"(pq1));
    SB();
    const f4 s = (__builtin_bit_cast(f4, t[0]) + __builtin_bit_cast(f4, t[1])) + (__builtin_bit_cast(f4, t[2]) + __builtin_bit_cast(f4, t[3]));
    *(LAS f4*)(Ll + L_CONS + w * 1024) = s;
    f4 z = __builtin_bit_cast(f4, zt);
#pragma unroll
    for (int r = 0; r < 4; ++r) {
      z[r] += asf(__builtin_amdgcn_mov_dpp(asu(z[r]), 0x124, 0xf, 0xf, true));
      z[r] += asf(__builtin_amdgcn_mov_dpp(asu(z[r]), 0x128, 0xf, 0xf, true));
    }
    if (lane < 4) *(LAS f4*)(L + L_ZC + w * 64 + lane * 16) = z;
  }
  BAR();
  STAMP(7);
  {
    f4 k0 = {0, 0, 0, 0}, k1 = k0, z0 = k0, z1 = k0;
#pragma unroll
    for (int n = 0; n < 4; ++n) {
      k0 += *(LAS f4*)(Ll + L_CONS + (2 * n) * 1024);
      k1 += *(LAS f4*)(Ll + L_CONS + (2 * n + 1) * 1024);
      z0 += *(LAS f4*)(L + L_ZC + (2 * n) * 64 + g * 16);
      z1 += *(LAS f4*)(L + L_ZC + (2 * n + 1) * 64 + g * 16);
    }
    const f4 z4 = {0, 0, 0, 0};
    const f4 n0 = mfma16(v2u{pk(k0[0], k0[1]), pk(k0[2], k0[3])}, pq0, z4), n1 = mfma16(v2u{pk(k1[0], k1[1]), pk(k1[2], k1[3])}, pq1, z4);
    const f4 d0 = mfma16(v2u{pk(z0[0], z0[1]), pk(z0[2], z0[3])}, pq0, z4), d1 = mfma16(v2u{pk(z1[0], z1[1]), pk(z1[2], z1[3])}, pq1, z4);
    const float r0 = __builtin_amdgcn_rcpf(d0[0] + 1e-6f), r1 = __builtin_amdgcn_rcpf(d1[0] + 1e-6f);
    const v4u o = {nopoison(pk(n0[0] * r0, n0[1] * r0)), nopoison(pk(n0[2] * r0, n0[3] * r0)), nopoison(pk(n1[0] * r1, n1[1] * r1)), nopoison(pk(n1[2] * r1, n1[3] * r1))};
    st16(o, rWS, lane * 16, WS_O + ohoff);
  }
  if (w == 0) {
    for (unsigned sp = 0; sp < (1u << 15); ++sp) {
      const unsigned v = lane < 16 ? __hip_atomic_load(flags + (512 + p * 16 + i) * 32, RLX_AGENT) : tag;
      if (__all(v == tag)) break;
    }
    asm volatile("" ::: "memory");
  }
  const int grp2 = w < 5 ? w : w + 8;
  dma7(dW1, vW1, grp2 * (32 * 3584) + 224 * j, lds0 + L_W1 + grp2 * 7168);
  __builtin_amdgcn_s_barrier();
  STAMP(8);
#define LOAD1(s) f[s] = __builtin_amdgcn_raw_buffer_load_b128(rWS, lane * 16 + ((s) & 3) * 1024, so + ((s) >> 2) * 4096, SC1)
#define LOAD16() do { _Pragma("unroll") for (int s = 0; s < PD; ++s) { LOAD1(s); SB(); } } while (0)
  {
    f4 a0, a1;
    const int so = WS_O + (p * 8 + w) * 16384;
    v4u f[16];
    LAS char* const o0 = opq(L + L_WO + (4 * g + q) * 64 + pp * 8 + (g & 1) * 32);
    LAS char* const o1 = opq(L + L_WO + (4 * g + q) * 64 + pp * 8 + ((g & 1) ^ 1) * 32);
    LOAD16();
    repack224(L + L_W1 + (5 + w) * 7168, rd1, lane);
#pragma nounroll
    for (unsigned tries = 0; tries < 4096; ++tries) {
      a0 = f4{0, 0, 0, 0}; a1 = a0;
      v4u wf[3][2];
#pragma unroll
      for (int s = 0; s < 2; ++s) { wf[s][0] = trfrag(o0 + s * 2048, 1024); wf[s][1] = trfrag(o1 + s * 2048, 1024); }
#pragma unroll
      for (int s = 0; s < 16; ++s) {
        if (s + 2 < 16) { wf[(s + 2) % 3][0] = trfrag(o0 + (s + 2) * 2048, 1024); wf[(s + 2) % 3][1] = trfrag(o1 + (s + 2) * 2048, 1024); }
        a0 = mfma32(wf[s % 3][0], f[s], a0);
        a1 = mfma32(wf[s % 3][1], f[s], a1);
        if (s + PD < 16) LOAD1(s + PD < 16 ? s + PD : 0);
        SB();
      }
      if (__all(a0[0] == a0[0])) break;
      unsigned m = 0;
#pragma unroll
      for (int s = 0; s < 16; ++s) m = fragmax(m, f[s]);
      if (__all(m != POISON)) break;
      COUNT(21);
      __builtin_amdgcn_s_sleep(4);
      asm volatile("" ::: "memory");
      LOAD16();
    }
    const v4u h = {nopoison(pk(a0[0] + bflo(xr0.x), a0[1] + bfhi(xr0.x))), nopoison(pk(a0[2] + bflo(xr0.y), a0[3] + bfhi(xr0.y))),
                   nopoison(pk(a1[0] + bflo(xr1.x), a1[1] + bfhi(xr1.x))), nopoison(pk(a1[2] + bflo(xr1.y), a1[3] + bfhi(xr1.y)))};
    st16(h, rWS, lane * 16, WS_H + ohoff);
    if (bid == 0 && tid == 0) __builtin_amdgcn_raw_buffer_store_b32(tag, rWS, WS_EPOCH, 0, SC1);
  }
  STAMP(9);
  repack224(L + L_W1 + grp2 * 7168, rd1, lane);
  const int th = j >> 3, c4 = j & 7;
  v4u w2r[7][4];
  const int vW2 = (((lane >> 3) & 1) * 16 + g * 4) * 1024 + (lane & 7) * 16, sW2 = 224 * w * 1024 + c4 * 128;
#define W2LOAD(s) do { _Pragma("unroll") for (int n_ = 0; n_ < 4; ++n_) w2r[s][n_] = __builtin_amdgcn_raw_buffer_load_b128(rW2, vW2 + n_ * 1024, sW2 + (s) * 32768, 0); } while (0)
#pragma unroll
  for (int s = 0; s < W2B; ++s) W2LOAD(s);
  BAR();
  STAMP(10);
  v4u hres;
  {
    constexpr int NF = 5;
    v4u f[16];
    const int so = WS_H + (p * 8 + w) * 16384;
    LAS char* const wp0 = opq(Ll + L_W1 + (j & 1) * 1024);
    LAS char* const wp1 = opq(wp0 + 8 * 7168);
#define W1F(s, t) ldf(((s) < 8 ? wp0 : wp1) + ((s) & 7) * 7168 + (t) * 1024)
    f4 acc[7];
    LOAD16();
    if (w < 7) {
      const float c = colsum(Ll + L_W1 + w * 1024, 7168);
      if (g == 0) *(LAS float*)(L + L_C1 + (w * 16 + i) * 4) = c;
    }
#pragma nounroll
    for (unsigned tries = 0; tries < 4096; ++tries) {
#pragma unroll
      for (int t = 0; t < NF; ++t) acc[t] = f4{0, 0, 0, 0};
      sm = acc[0]; gr = acc[0];
      v4u wf[4];
#pragma unroll
      for (int n = 0; n < 3; ++n) wf[n] = W1F(n / NF, n % NF);
#pragma unroll
      for (int n = 0; n < 16 * NF; ++n) {
        if (n + 3 < 16 * NF) wf[(n + 3) & 3] = W1F((n + 3) / NF, (n + 3) % NF);
        acc[n % NF] = mfma32(wf[n & 3], f[n / NF], acc[n % NF]);
        const v4u a = f[n / NF];
        if (n % NF == 0) sm = mfma32(ones, a, sm);
        if (n % NF == 2) gr = mfma32(a, a, gr);
        if (n % NF == 1 && n / NF + PD < 16) LOAD1(n / NF + PD < 16 ? n / NF + PD : 0);
        SB();
      }
      asm volatile("" : "+v"(sm), "+v"(gr));
      if (__all(acc[0][0] == acc[0][0])) break;
      unsigned m = 0;
#pragma unroll
      for (int s = 0; s < 16; ++s) m = fragmax(m, f[s]);
      if (__all(m != POISON)) break;
      COUNT(22);
      __builtin_amdgcn_s_sleep(4);
      asm volatile("" ::: "memory");
      LOAD16();
    }
    const float sq = asf(__builtin_amdgcn_ds_bpermute((16 * q + i) * 4, asu(pp == 0 ? gr[0] : pp == 1 ? gr[1] : pp == 2 ? gr[2] : gr[3])));
    const float mean2 = sm[0] * (1.f / 512.f);
    float rstd2 = __builtin_amdgcn_rsqf(fmaxf(sq * (1.f / 512.f) - mean2 * mean2, 0.f) + 1e-5f);
    float nm = -mean2 * rstd2;
    asm volatile("" : "+v"(nm), "+v"(rstd2));
    STAMP(11);
    BAR();
    STAMP(12);
    const int aoff = WS_A + ((p * 8 + w) * 56 + ((7 * j + (j & 1)) >> 1)) * 1024;
    LAS char* const cp = L + L_C1 + ((j & 1) * 16 + 4 * g) * 4;
    LAS char* const cs = L + L_C1 + (((j & 1) ? 0 : 96) + 4 * g) * 4;
    LAS char* const ws0 = opq(Ll + L_W1 + ((j & 1) ? 0 : 6144));
    LAS char* const ws1 = opq(ws0 + 8 * 7168);
#define W1S(s) ldf(((s) < 8 ? ws0 : ws1) + ((s) & 7) * 7168)
    acc[5] = f4{0, 0, 0, 0}; acc[6] = acc[5];
    f4 cq[2], bq[2];
    v4u wf[2][4];
    cq[0] = *(LAS f4*)cp; bq[0] = *(LAS f4*)(cp + (L_B1 - L_C1));
    wf[0][0] = W1F(0, 5); wf[0][1] = W1S(0); wf[0][2] = W1F(1, 5); wf[0][3] = W1S(1);
    hres = __builtin_amdgcn_raw_buffer_load_b128(rWS, lane * 16, WS_H + ((p * 8 + 4 * th + (w >> 1)) * 16 + 2 * c4 + (w & 1)) * 1024, SC1);
    unsigned e[14];
#pragma unroll
    for (int k = 0; k < 14; ++k) {
      const int t = k >> 1, hf = k & 1;
      if (hf == 0 && t < 6) { LAS char* const cc = t + 1 < 6 ? cp + (t + 1) * 64 : cs; cq[(t + 1) & 1] = *(LAS f4*)cc; bq[(t + 1) & 1] = *(LAS f4*)(cc + (L_B1 - L_C1)); }
      if (k + 1 < 8) { wf[(k + 1) & 1][0] = W1F(2 * k + 2, 5); wf[(k + 1) & 1][1] = W1S(2 * k + 2); wf[(k + 1) & 1][2] = W1F(2 * k + 3, 5); wf[(k + 1) & 1][3] = W1S(2 * k + 3); }
      if (k < 4 * W2C) w2r[W2B + (k < 4 * W2C ? k : 0) / 4][k & 3] = __builtin_amdgcn_raw_buffer_load_b128(rW2, vW2 + (k & 3) * 1024, sW2 + (W2B + k / 4) * 32768, 0);
      if (k < 8) {
        acc[5] = mfma32(wf[k & 1][0], f[2 * k], acc[5]);
        acc[6] = mfma32(wf[k & 1][1], f[2 * k], acc[6]);
        acc[5] = mfma32(wf[k & 1][2], f[2 * k + 1], acc[5]);
        acc[6] = mfma32(wf[k & 1][3], f[2 * k + 1], acc[6]);
      }
      const float y0 = mish(acc[t][2 * hf] * rstd2 + (nm * cq[t & 1][2 * hf] + bq[t & 1][2 * hf]));
      const float y1 = mish(acc[t][2 * hf + 1] * rstd2 + (nm * cq[t & 1][2 * hf + 1] + bq[t & 1][2 * hf + 1]));
      e[k] = pk(y0, y1);
      if ((k & 3) == 3) st16(v4u{e[k - 3], e[k - 2], e[k - 1], e[k]}, rWS, lane * 16 + (k >> 2) * 1024, aoff);
      if (k == 13) __builtin_amdgcn_raw_buffer_store_b64(v2u{e[12], e[13]}, rWS, lane * 16 + (j & 1) * 8, aoff + ((j & 1) ? -1024 : 3072), SC1);
      SB();
    }
  }
  STAMP(13);
#pragma unroll
  for (int s = 0; s < W2B; ++s) { w2pack(w2r[s]); SB(); }
  VMWAIT(0);
  BAR();
  if (tid == 0) __hip_atomic_store(flags + (256 + p * 16 + j) * 32, tag, RLX_AGENT);
  STAMP(14);
#pragma unroll
  for (int s = W2B + W2C; s < 7; ++s) W2LOAD(s);
  SB();
  *(LAS v4u*)(Ll + L_HRES + w * 1024) = hres;
#pragma unroll
  for (int s = W2B; s < W2B + W2C; ++s) { w2pack(w2r[s]); SB(); }
  {
    GAS unsigned* const fp = flags + (256 + p * 16 + 2 * w + (lane & 1)) * 32;
    for (unsigned sp = 0; sp < (1u << 15); ++sp) {
      const unsigned v = lane < 2 ? __hip_atomic_load(fp, RLX_AGENT) : tag;
      if (__all(v == tag)) break;
      COUNT(23);
    }
    asm volatile("" ::: "memory");
  }
  STAMP(15);
  {
    const int so = WS_A + ((p * 8 + 4 * th) * 56 + 7 * w) * 1024;
    constexpr int NB = PD + 1;
    v4u f[NB];
#define ALOAD(n) __builtin_amdgcn_raw_buffer_load_b128(rWS, lane * 16 + (((n) % 7) & 3) * 1024, so + (((n) / 7) * 56 + (((n) % 7) & 4)) * 1024, SC1)
#pragma unroll
    for (int n = 0; n < NB; ++n) { f[n] = ALOAD(n); SB(); }
#pragma unroll
    for (int s = W2B + W2C; s < 7; ++s) { w2pack(w2r[s]); SB(); }
    LAS char* const wr = L + (w * 1024 + 16 * g + (i ^ (4 * g))) * 16;
    f4 acc[4];
#pragma unroll
    for (int n = 0; n < 28; ++n) {
      const int s = n % 7, t = n / 7;
#pragma unroll
      for (int u = 0; u < 4; ++u) acc[u] = mfma32(w2r[s][u], f[n % NB], s ? acc[u] : f4{0, 0, 0, 0});
      if (n + NB < 28) f[n % NB] = ALOAD(n + NB < 28 ? n + NB : 0);
      if (s == 6) {
#pragma unroll
        for (int u = 0; u < 4; ++u) *(LAS f4*)(wr + (t * 4 + u) * 1024) = acc[u];
      }
      SB();
    }
    STAMP(16);
    BAR();
    STAMP(17);
    int tid2 = w * 64 + lane;
    asm volatile("" : "+v"(tid2));
    const int tok = tid2 >> 3, pc = tid2 & 7, gg = (pc >> 2) + 2 * (pc & 1);
    LAS char* rp = L + (((tok >> 4) * 4) * 64 + 16 * gg + ((tok & 15) ^ (4 * gg))) * 16 + ((pc >> 1) & 1) * 8;
    LAS char* const rq = opq(rp + 65536);
    LAS char* const hp = L + L_HRES + ((tok >> 4) * 2 + (pc >> 2)) * 1024 + (16 * (pc & 1) + (tok & 15)) * 16 + ((pc >> 1) & 1) * 8;
    const v2u hr0 = *(volatile LAS v2u*)hp, hr1 = *(volatile LAS v2u*)(hp + 512);
    f2 r[4];
#pragma unroll
    for (int hh = 0; hh < 2; ++hh) {
      f2 t[16];
#pragma unroll
      for (int n = 0; n < 16; ++n) t[n] = *(volatile LAS f2*)((hh ? rq : rp) + (n >> 2) * 16384 + (n & 3) * 1024);
#pragma unroll
      for (int u = 0; u < 4; ++u) {
        const f2 s = (t[u] + t[4 + u]) + (t[8 + u] + t[12 + u]);
        r[u] = hh ? r[u] + s : s;
      }
    }
    const v2u o0 = {pk(r[0][0] + bflo(hr0.x), r[1][0] + bfhi(hr0.x)), pk(r[2][0] + bflo(hr0.y), r[3][0] + bfhi(hr0.y))};
    const v2u o1 = {pk(r[0][1] + bflo(hr1.x), r[1][1] + bfhi(hr1.x)), pk(r[2][1] + bflo(hr1.y), r[3][1] + bfhi(hr1.y))};
    u16* op = out + (size_t)(p * 128 + th * 64 + tok) * 512 + 64 * c4 + 32 * (pc >> 2) + 16 * ((pc >> 1) & 1) + 4 * (pc & 1);
    *(v2u*)op = o0;
    *(v2u*)(op + 8) = o1;
  }
  STAMP(18);
  STAMP_FLUSH
}

extern "C" void kernel_launch(void* const* d_in, const int* in_sizes, int n_in, void* d_out, int out_size, void* d_ws, size_t ws_size,
                              hipStream_t stream) {
  (void)in_sizes; (void)n_in; (void)out_size; (void)ws_size;
  (void)hipFuncSetAttribute((const void*)fused_block, hipFuncAttributeMaxDynamicSharedMemorySize, LDS_BYTES);
  fused_block<<<256, 512, LDS_BYTES, stream>>>((const u16*)d_in[0], (const u16*)d_in[1], (const u16*)d_in[2], (const u16*)d_in[4],
                                               (const u16*)d_in[5], (const u16*)d_in[6], (u16*)d_out, (char*)d_ws);
}
```

```cpp
#include <hip/hip_runtime.h>
#include <hip/hip_bf16.h>

typedef unsigned short u16;
typedef unsigned v4u __attribute__((ext_vector_type(4)));
typedef unsigned v2u __attribute__((ext_vector_type(2)));
typedef float f4 __attribute__((ext_vector_type(4)));
typedef float f2 __attribute__((ext_vector_type(2)));
typedef short s4 __attribute__((ext_vector_type(4)));
typedef __bf16 b8 __attribute__((ext_vector_type(8)));
typedef __bf16 b2 __attribute__((ext_vector_type(2)));
#define GAS __attribute__((address_space(1)))
#define LAS __attribute__((address_space(3)))
#if defined(__HIP_DEVICE_COMPILE__)
#define TGT __attribute__((target("no-packed-fp32-ops")))
#else
#define TGT
#endif
#define DI __device__ __forceinline__ TGT
#define RLX_AGENT __ATOMIC_RELAXED, __HIP_MEMORY_SCOPE_AGENT

#define MAPMODE 1
#define XP 0
#define PD 6
#define KVAT 12
#define W2A 4

constexpr int WS_EPOCH = 0;
constexpr int WS_FLAG = 4096;
constexpr int WS_XS = 196608;
constexpr int WS_KV = 262144;
constexpr int KV_REC = 1152;
constexpr int WS_O = 1 << 20;
constexpr int WS_H = 3 << 20;
constexpr int WS_A = 5 << 20;
constexpr int WS_S = 12 << 20;
constexpr int WS_P1 = 13 << 20;
constexpr int WS_PO = 15 << 20;
constexpr int WS_PC = WS_PO + (1 << 19);
constexpr int WS_P2 = 16 << 20;
constexpr int WS_BYTES = 18 << 20;
#define STAMP(n)
#define STAMP_DECL
#define STAMP_FLUSH
#define COUNT(n)

constexpr int L_WQ = 0, L_WK = 32768, L_KVRED = 98304, L_WO = 114688, L_SCR = 147456;
constexpr int L_RAW = 0;
constexpr int L_W1 = 0;
constexpr int L_CONS = L_SCR;
constexpr int L_ZP = L_SCR + 8192;
constexpr int L_ZC = L_ZP + 512;
constexpr int L_CKV = L_ZC + 512;
constexpr int L_STT = L_CKV + 512;
constexpr int L_C1 = L_STT + 1024;
constexpr int L_B1 = L_C1 + 512;
constexpr int LDS_BYTES = 163840;

constexpr unsigned ONE2 = 0x3F803F80u;
constexpr int SC1 = 16;
constexpr float LOG2E = 1.4426950408889634f;
constexpr unsigned SPIN = 1u << 15;

DI float asf(unsigned u) { return __builtin_bit_cast(float, u); }
DI unsigned asu(float f) { return __builtin_bit_cast(unsigned, f); }
DI f4 mfma32(v4u a, v4u b, f4 c) { return __builtin_amdgcn_mfma_f32_16x16x32_bf16(__builtin_bit_cast(b8, a), __builtin_bit_cast(b8, b), c, 0, 0, 0); }
DI f4 mfma16(v2u a, v2u b, f4 c) { return __builtin_amdgcn_mfma_f32_16x16x16bf16_1k(__builtin_bit_cast(s4, a), __builtin_bit_cast(s4, b), c, 0, 0, 0); }
DI unsigned pk(float lo, float hi) { f2 v = {lo, hi}; return __builtin_bit_cast(unsigned, __builtin_convertvector(v, b2)); }
DI float bflo(unsigned u) { return asf(u << 16); }
DI float bfhi(unsigned u) { return asf(u & 0xffff0000u); }
DI v2u trd(LAS char* p) { return __builtin_bit_cast(v2u, __builtin_amdgcn_ds_read_tr16_b64_v4i16((LAS s4*)p)); }
DI v4u trfrag(LAS char* p, int second) { v2u a = trd(p), b = trd(p + second); return v4u{a.x, a.y, b.x, b.y}; }
DI v4u ldf(LAS char* p) { return *(LAS v4u*)p; }
DI float dot2(unsigned a, unsigned b, float c) { return __builtin_amdgcn_fdot2_f32_bf16(__builtin_bit_cast(b2, a), __builtin_bit_cast(b2, b), c, false); }
template <int C> DI float dppadd(float v) { return v + asf(__builtin_amdgcn_mov_dpp(asu(v), C, 0xf, 0xf, true)); }
DI float rowsum4(float v) {
  auto r = __builtin_amdgcn_permlane32_swap(asu(v), asu(v), false, false);
  const float s = asf(r[0]) + asf(r[1]);
  auto t = __builtin_amdgcn_permlane16_swap(asu(s), asu(s), false, false);
  return asf(t[0]) + asf(t[1]);
}
DI bool none(bool c) { return __builtin_amdgcn_ballot_w64(c) == 0; }
DI v4u mksrd(const void* p, unsigned bytes) {
  unsigned long long a = (unsigned long long)p;
  return v4u{(unsigned)a, (unsigned)(a >> 32) & 0xffffu, bytes, 0x00020000u};
}
DI void dma16(v4u srd, unsigned voff, unsigned soff, unsigned lds) {
  asm volatile("s_mov_b32 m0, %3\n\ts_nop 0\n\tbuffer_load_dwordx4 %0, %1, %2 offen lds" ::"v"(voff), "s"(srd), "s"(soff), "s"(lds) : "memory");
}
template <int N> DI void dmac(v4u srd, unsigned voff, unsigned so, unsigned lds) {
  if (N == 1) asm volatile("s_mov_b32 m0, %3\n\ts_nop 0\n\tbuffer_load_dwordx4 %0, %1, %2 offen sc1 lds" ::"v"(voff), "s"(srd), "s"(so), "s"(lds) : "memory");
  if (N == 3) asm volatile("s_mov_b32 m0, %3\n\ts_nop 0\n\tbuffer_load_dwordx4 %0, %1, %2 offen sc1 lds\n\tbuffer_load_dwordx4 %0, %1, %2 offen offset:1024 sc1 lds\n\t"
                           "buffer_load_dwordx4 %0, %1, %2 offen offset:2048 sc1 lds" ::"v"(voff), "s"(srd), "s"(so), "s"(lds) : "memory");
  if (N == 4) asm volatile("s_mov_b32 m0, %3\n\ts_nop 0\n\tbuffer_load_dwordx4 %0, %1, %2 offen sc1 lds\n\tbuffer_load_dwordx4 %0, %1, %2 offen offset:1024 sc1 lds\n\t"
                           "buffer_load_dwordx4 %0, %1, %2 offen offset:2048 sc1 lds\n\tbuffer_load_dwordx4 %0, %1, %2 offen offset:3072 sc1 lds"
                           ::"v"(voff), "s"(srd), "s"(so), "s"(lds) : "memory");
}
#define LD16(dst, srd, voff, soff) asm volatile("buffer_load_dwordx4 %0, %1, %2, %3 offen sc1" : "=v"(dst) : "v"(voff), "s"(srd), "s"(soff) : "memory")
DI void st16(v4u v, __amdgpu_buffer_rsrc_t r, int voff, int soff) {
  __builtin_amdgcn_raw_buffer_store_b128(v, r, voff, soff, SC1);
  asm volatile("s_nop 1" ::"v"(v) : "memory");
}
DI LAS char* opq(LAS char* p) { asm volatile("" : "+v"(p)); return p; }
#define LGKM0() asm volatile("s_waitcnt lgkmcnt(0)" ::: "memory")
#define SBAR() do { __builtin_amdgcn_s_barrier(); asm volatile("" ::: "memory"); } while (0)
#define BAR() do { LGKM0(); SBAR(); } while (0)
#define VMWAIT(n) asm volatile("s_waitcnt vmcnt(%0)" ::"i"(n) : "memory")
#define SB() __builtin_amdgcn_sched_barrier(0)

DI float phi(float t) { return fmaxf(t, 0.f) + __builtin_amdgcn_exp2f(fminf(t, 0.f) * LOG2E) + 1e-6f; }
DI void poll(GAS unsigned* fp, unsigned tag, int n, int lane) {
#pragma nounroll
  for (unsigned sp = 0; sp < SPIN; ++sp) {
    const unsigned v = lane < n ? __hip_atomic_load(fp, RLX_AGENT) : tag;
    if (none(v != tag)) break;
  }
  asm volatile("" ::: "memory");
}

__global__ void __launch_bounds__(512, 2) TGT
fused_block(const u16* __restrict__ x, const u16* __restrict__ wqkv, const u16* __restrict__ wo, const u16* __restrict__ w1,
            const u16* __restrict__ b1, const u16* __restrict__ w2, u16* __restrict__ out, char* ws) {
  extern __shared__ __attribute__((aligned(16))) char smem_[];
  LAS char* L = (LAS char*)smem_;
  const int tid = threadIdx.x, lane = tid & 63, w = __builtin_amdgcn_readfirstlane(tid >> 6);
  const int g = lane >> 4, i = lane & 15, q = (lane >> 2) & 3, pp = lane & 3;
  const int bid = blockIdx.x, xcd = bid & 7, rk = bid >> 3;
  const int p = 4 * (xcd >> 1) + (rk >> 3), j = (xcd & 1) * 8 + (rk & 7);
  const int rot = 2 * p + j;
  STAMP_DECL
  STAMP(0);
  const v4u dQKV = mksrd(wqkv, 512 * 1536 * 2), dX = mksrd(x, 2048 * 512 * 2), dWS = mksrd(ws, WS_BYTES);
  const __amdgpu_buffer_rsrc_t rWS = __builtin_amdgcn_make_buffer_rsrc(ws, 0, WS_BYTES, 0x00020000);
  const unsigned lds0 = (unsigned)(size_t)L;
  LAS char* const Ll = L + lane * 16;
  const int th = j >> 3, c4 = j & 7;

  const unsigned vW = (lane >> 2) * 3072 + (pp ^ ((lane >> 5) << 1)) * 16;
  const unsigned vX0 = (lane >> 3) * 1024 + ((lane & 7) ^ g) * 16, vX1 = (vX0 ^ 64) + 8192;
  const unsigned sX = (p * 128 + w * 16) * 1024;
  const unsigned sW = (w & 3) * (16 * 3072) + (1 + (w >> 2)) * 1024 + j * 64, lW = lds0 + L_WK + (w >> 2) * 32768 + (w & 3) * 1024;
#define XSLOT(c) ((c) % 3 == 0 ? L_KVRED + w * 2048 : L_WO + w * 4096 + ((c) % 3 - 1) * 2048)
#define WLOAD(c) dma16(dQKV, vW, sW + (((c) + rot) & 7) * (64 * 3072), lW + (c) * 4096)
#define XLOAD1(c, h) dma16(dX, (h) ? vX1 : vX0, sX + (((c) + rot) & 7) * 128, lds0 + XSLOT(c) + (h) * 1024)
#define QLOAD1(n) dma16(dQKV, vW, (((w + rot) & 7) * 64 + (n) * 16) * 3072 + j * 64, lds0 + L_WQ + w * 4096 + (n) * 1024)
  unsigned tag;
  v4u xrow;
  asm volatile("buffer_load_dwordx4 %0, %1, %2, %3 offen" : "=v"(xrow) : "v"(lane * 16), "s"(dX), "s"((p * 128 + j * 8 + w) * 1024) : "memory");
  asm volatile("buffer_load_dword %0, off, %1, 0 sc1" : "=v"(tag) : "s"(dWS) : "memory");
  WLOAD(0); XLOAD1(0, 0); XLOAD1(0, 1);
  SBAR();
  WLOAD(1); XLOAD1(1, 0); XLOAD1(1, 1);
  const int u2 = 2 * p + th + 32 * (w >> 2);
  {
    const unsigned c = w * 64 + lane, r = (c * 4682u) >> 16;
    if (w < 7) dma16(mksrd(w1, 512 * 1792 * 2), r * 3584 + (c - r * 14) * 16, p * (32 * 3584) + 224 * j, lds0 + L_RAW + w * 1024);
    if (w >= 6) dma16(mksrd(wo, 512 * 512 * 2), (lane >> 2) * 1024 + pp * 16, (p * 32 + (w & 1) * 16) * 1024 + j * 64, lds0 + L_RAW + 7168 + (w & 1) * 1024);
    dma16(mksrd(w2, 1792 * 512 * 2), (lane >> 3) * 1024 + (lane & 7) * 16, (u2 * 32 + (w & 3) * 8) * 1024 + c4 * 128, lds0 + L_RAW + 9216 + w * 1024);
  }
  STAMP(1);
  asm volatile("s_waitcnt vmcnt(8)" : "+v"(xrow), "+v"(tag) :: "memory");
  tag += 1u;
  {
    float s = dot2(xrow.x, ONE2, dot2(xrow.y, ONE2, 0.f)) + dot2(xrow.z, ONE2, dot2(xrow.w, ONE2, 0.f));
    float ss = dot2(xrow.x, xrow.x, dot2(xrow.y, xrow.y, 0.f)) + dot2(xrow.z, xrow.z, dot2(xrow.w, xrow.w, 0.f));
    s = rowsum4(dppadd<0x128>(dppadd<0x124>(dppadd<0x122>(dppadd<0x121>(s))))) * (1.f / 512.f);
    ss = rowsum4(dppadd<0x128>(dppadd<0x124>(dppadd<0x122>(dppadd<0x121>(ss))))) * (1.f / 512.f);
    const float r = __builtin_amdgcn_rsqf(fmaxf(ss - s * s, 0.f) + 1e-5f);
    if (lane == 0) st16(v4u{asu(s), tag, asu(r), tag}, rWS, 0, WS_XS + (p * 128 + j * 8 + w) * 16);
  }
  GAS unsigned* const flags = (GAS unsigned*)(ws + WS_FLAG);
  GAS unsigned* const myflag = flags + (p * 16 + j) * 32;
  const int hd = w & 1;
  LAS char* const tN = L + (8 * g + q) * 64 + pp * 8 + ((g ^ hd) & 1) * 32;
  LAS char* const kN = opq(tN + L_WK);
  LAS char* const kC = opq(tN + L_WK + ((w >> 1) & 1) * 32768 + (w >> 2) * 2048);
#define WFRAG(s, m) trfrag(kN + (m) * 32768 + (s) * 2048, 256)
  v4u ones = {ONE2, ONE2, ONE2, ONE2};
  asm volatile("" : "+v"(ones));
  v4u xf[2][16], xres;
  const f4 z4 = {0, 0, 0, 0};
  f4 ak[2] = {z4, z4}, av[2] = {z4, z4}, cs = z4;
  const int xrd = i * 128 + (g ^ (i >> 1)) * 16;
  LAS char* xp[2][2][2];
#pragma unroll
  for (int o = 0; o < 2; ++o)
#pragma unroll
    for (int h = 0; h < 2; ++h) { xp[o][0][h] = L + L_KVRED + (w ^ o) * 2048 + (xrd ^ (h * 64)); xp[o][1][h] = L + L_WO + (w ^ o) * 4096 + (xrd ^ (h * 64)); }
#define XRD(o, c, h) xf[o][2 * (c) + (h)] = ldf((c) % 3 ? xp[o][1][h] + ((c) % 3 - 1) * 2048 : xp[o][0][h])
  v4u wf[2][4], cf, xs, pr1, pro, pr2;
  f4 prc;
  f4 mr[2], rs[2];
  f2 c2k, c2v;
  const int cres = ((j >> 1) - rot) & 7;
  const int rot4 = (j & 3) * 4;
#define P1L(N, slot, n) if (!(XP & 2)) dmac<N>(dWS, lane * 16, WS_P1 + (j * 112 + (((slot) + rot4) & 15) * 7 + (n)) * 1024, lds0 + L_W1 + ((slot) * 7 + (n)) * 1024)
#pragma unroll
  for (int c = -1; c < 8; ++c) {
    if (c >= 0) {
      ak[0] = mfma32(xf[0][2 * c], wf[c & 1][0], ak[0]);
      ak[1] = mfma32(xf[1][2 * c], wf[c & 1][0], ak[1]);
      SB();
    }
    if (c < 6) VMWAIT(c < 1 ? 5 : 3);
    else if (c == 6) {
      asm volatile("s_waitcnt vmcnt(2)" : "+v"(xs) :: "memory");
#pragma nounroll
      for (unsigned sp = 0; sp < SPIN && !none(xs.y != tag || xs.w != tag); ++sp) {
        COUNT(23);
        xs = __builtin_amdgcn_raw_buffer_load_b128(rWS, i * 16, WS_XS + (p * 128 + w * 16) * 16, SC1);
        asm volatile("" ::: "memory");
      }
      if (g == 0) { *(LAS unsigned*)(L + L_STT + w * 128 + i * 4) = xs.x; *(LAS unsigned*)(L + L_STT + w * 128 + 64 + i * 4) = xs.z; }
    } else LGKM0();
    SBAR();
    if (c >= 0) {
      av[0] = mfma32(xf[0][2 * c], wf[c & 1][1], av[0]);
      av[1] = mfma32(xf[1][2 * c], wf[c & 1][1], av[1]);
    }
    if (c < 7) {
      XRD(0, c + 1, 0); XRD(1, c + 1, 0); XRD(0, c + 1, 1); XRD(1, c + 1, 1);
      cf = trfrag(kC + (c + 1) * 4096, 256);
    } else {
#pragma unroll
      for (int o = 0; o < 2; ++o) { mr[o] = *(LAS f4*)(L + L_STT + (w ^ o) * 128 + g * 16); rs[o] = *(LAS f4*)(L + L_STT + (w ^ o) * 128 + 64 + g * 16); }
      c2k = *(LAS f2*)(L + L_CKV + (hd * 16 + i) * 8); c2v = *(LAS f2*)(L + L_CKV + (32 + hd * 16 + i) * 8);
    }
    if (c < 5) WLOAD(c + 3);
    if (c == 5) LD16(xs, dWS, i * 16, WS_XS + (p * 128 + w * 16) * 16);
    SB();
    if (c >= 0) {
      ak[0] = mfma32(xf[0][2 * c + 1], wf[c & 1][2], ak[0]);
      ak[1] = mfma32(xf[1][2 * c + 1], wf[c & 1][2], ak[1]);
    }
    if (c < 7) { wf[(c + 1) & 1][0] = WFRAG(2 * c + 2, 0); wf[(c + 1) & 1][1] = WFRAG(2 * c + 2, 1); }
    if (c < 5) XLOAD1(c + 3, 0); else if (c < 7) QLOAD1(2 * c - 10);
    SB();
    if (c >= 0) {
      av[0] = mfma32(xf[0][2 * c + 1], wf[c & 1][3], av[0]);
      av[1] = mfma32(xf[1][2 * c + 1], wf[c & 1][3], av[1]);
    }
    if (c < 7) { wf[(c + 1) & 1][2] = WFRAG(2 * c + 3, 0); wf[(c + 1) & 1][3] = WFRAG(2 * c + 3, 1); }
    if (c < 5) XLOAD1(c + 3, 1); else if (c < 7) QLOAD1(2 * c - 9);
    if (c == 7) {
      st16(pr1, rWS, lane * 16, w < 7 ? WS_P1 + ((j * 16 + p) * 7 + w) * 1024 : WS_BYTES);
      st16(pr2, rWS, lane * 16, u2 < 56 ? WS_P2 + ((c4 * 56 + u2) * 4 + (w & 3)) * 1024 : WS_BYTES);
      if (i == 0) st16(__builtin_bit_cast(v4u, prc), rWS, g * 16, w < 7 ? WS_PC + ((j * 7 + w) * 16 + p) * 64 : WS_BYTES);
      if (w >= 6) st16(pro, rWS, lane * 16, WS_PO + ((j * 16 + p) * 2 + (w & 1)) * 1024);
    }
    SB();
    if (c < 7) {
      cs = mfma32(ones, cf, cs);
      if (c + 1 == cres) xres = (j & 1) ? xf[0][2 * c + 3] : xf[0][2 * c + 2];
    }
    if (c == 1) {
      const int il = (pp * 2 + (w & 1)) * 8;
      pr1 = trfrag(L + L_RAW + (8 * g + q) * 224 + pp * 8 + w * 32, 4 * 224);
      pro = trfrag(L + L_RAW + 7168 + (8 * g + q) * 64 + il, 256);
      pr2 = trfrag(L + L_RAW + 9216 + (w >> 2) * 4096 + (4 * g + q) * 128 + (w & 2) * 32 + il, 2048);
    }
    if (c == 2) prc = mfma32(pr1, ones, z4);
    if (c == 6 && g == 0) *(LAS float*)(L + L_CKV + ((w & 3) * 16 + i) * 8 + (w >> 2) * 4) = cs[0];
  }
  STAMP(3);
  {
    const float ck = c2k[0] + c2k[1], cv = c2v[0] + c2v[1];
    float kk[8], vv[8];
#pragma unroll
    for (int n = 0; n < 8; ++n) {
      kk[n] = phi((ak[n >> 2][n & 3] - mr[n >> 2][n & 3] * ck) * rs[n >> 2][n & 3]);
      vv[n] = (av[n >> 2][n & 3] - mr[n >> 2][n & 3] * cv) * rs[n >> 2][n & 3];
    }
    const v4u pkf = {pk(kk[0], kk[1]), pk(kk[2], kk[3]), pk(kk[4], kk[5]), pk(kk[6], kk[7])};
    const v4u pvf = {pk(vv[0], vv[1]), pk(vv[2], vv[3]), pk(vv[4], vv[5]), pk(vv[6], vv[7])};
    const f4 kv = mfma32(pkf, pvf, z4), z = mfma32(pkf, ones, z4);
    *(LAS f4*)(Ll + L_KVRED + w * 1024) = kv;
    if (i == 0) *(LAS f4*)(L + L_ZP + w * 64 + g * 16) = z;
  }
  if (w < 6) VMWAIT(3); else VMWAIT(4);
  BAR();
  STAMP(4);
  if (w < 2) {
    __builtin_amdgcn_s_setprio(3);
    f4 t[8];
#pragma unroll
    for (int n = 0; n < 4; ++n) { t[n] = *(LAS f4*)(Ll + L_KVRED + (n * 2 + w) * 1024); t[4 + n] = *(LAS f4*)(L + L_ZP + (n * 2 + w) * 64 + pp * 16); }
    SB();
    const int so = WS_KV + ((j * 2 + w) * 16 + p) * KV_REC;
    const f4 a = (t[0] + t[1]) + (t[2] + t[3]), z = (t[4] + t[5]) + (t[6] + t[7]);
    st16(v4u{pk(a[0], a[1]), tag, pk(a[2], a[3]), tag}, rWS, lane * 16, so);
    if (lane < 4) st16(v4u{pk(z[0], z[1]), tag, pk(z[2], z[3]), tag}, rWS, lane * 16 + 1024, so);
    __builtin_amdgcn_s_setprio(0);
    STAMP(2);
  }
  v2u pq[2];
  v4u kt[5];
  const unsigned kso = WS_KV + ((j * 2 + hd) * 16 + 4 * (w >> 1)) * KV_REC;
  {
    f4 aq[2] = {z4, z4}, cq = z4;
    v4u qf[4];
#pragma unroll
    for (int s = 0; s < 3; ++s) qf[s] = trfrag(tN + L_WQ + s * 2048, 256);
#pragma unroll
    for (int s = 0; s < 16; ++s) {
      if (s + 3 < 16) qf[(s + 3) & 3] = trfrag(tN + L_WQ + (s + 3) * 2048, 256);
      aq[0] = mfma32(qf[s & 3], xf[0][s], aq[0]);
      aq[1] = mfma32(qf[s & 3], xf[1][s], aq[1]);
      cq = mfma32(qf[s & 3], ones, cq);
      if (s == 8) {
        if (w < 2) VMWAIT(2); else VMWAIT(0);
        SBAR();
        if (tid == 128) __hip_atomic_store(myflag, tag, RLX_AGENT);
      }
      if (s == KVAT) {
#pragma unroll
        for (int n = 0; n < 4; ++n) LD16(kt[n], dWS, lane * 16, kso + n * KV_REC);
        LD16(kt[4], dWS, q * KV_REC + pp * 16 + 1024, kso);
      }
      SB();
    }
#pragma unroll
    for (int o = 0; o < 2; ++o) {
      const float m = *(LAS float*)(L + L_STT + (w ^ o) * 128 + i * 4), r = *(LAS float*)(L + L_STT + (w ^ o) * 128 + 64 + i * 4);
      float a[4];
#pragma unroll
      for (int n = 0; n < 4; ++n) a[n] = phi((aq[o][n] - m * cq[n]) * r);
      pq[o] = v2u{pk(a[0], a[1]), pk(a[2], a[3])};
    }
    asm volatile("" : "+v"(pq[0]), "+v"(pq[1]));
  }
  STAMP(5);
  {
    asm volatile("s_waitcnt vmcnt(0)" : "+v"(kt[0]), "+v"(kt[1]), "+v"(kt[2]), "+v"(kt[3]), "+v"(kt[4]) :: "memory");
#pragma nounroll
    for (unsigned sp = 0; sp < SPIN; ++sp) {
      unsigned bad = 0;
#pragma unroll
      for (int n = 0; n < 5; ++n) bad |= (kt[n].y ^ tag) | (kt[n].w ^ tag);
      if (none(bad != 0)) break;
      COUNT(24);
#pragma unroll
      for (int n = 0; n < 4; ++n) kt[n] = __builtin_amdgcn_raw_buffer_load_b128(rWS, lane * 16, kso + n * KV_REC, SC1);
      kt[4] = __builtin_amdgcn_raw_buffer_load_b128(rWS, q * KV_REC + pp * 16 + 1024, kso, SC1);
      asm volatile("" ::: "memory");
    }
    STAMP(6);
    const f4 s = {(bflo(kt[0].x) + bflo(kt[1].x)) + (bflo(kt[2].x) + bflo(kt[3].x)), (bfhi(kt[0].x) + bfhi(kt[1].x)) + (bfhi(kt[2].x) + bfhi(kt[3].x)),
                  (bflo(kt[0].z) + bflo(kt[1].z)) + (bflo(kt[2].z) + bflo(kt[3].z)), (bfhi(kt[0].z) + bfhi(kt[1].z)) + (bfhi(kt[2].z) + bfhi(kt[3].z))};
    *(LAS f4*)(Ll + L_CONS + w * 1024) = s;
    const f4 z = {dppadd<0x128>(dppadd<0x124>(bflo(kt[4].x))), dppadd<0x128>(dppadd<0x124>(bfhi(kt[4].x))),
                  dppadd<0x128>(dppadd<0x124>(bflo(kt[4].z))), dppadd<0x128>(dppadd<0x124>(bfhi(kt[4].z)))};
    if (lane < 4) *(LAS f4*)(L + L_ZC + w * 64 + lane * 16) = z;
  }
  BAR();
  STAMP(7);
  unsigned pf = tag;
  if (w == 0) asm volatile("buffer_load_dword %0, %1, %2, %3 offen sc1" : "+v"(pf) : "v"((i * 16 + (j ^ ((g & 1) << 3))) * 128), "s"(dWS), "s"(WS_FLAG) : "memory");
  const int ohoff = ((p * 8 + w) * 16 + j) * 1024;
  {
    f4 kq[4], zq[4];
#pragma unroll
    for (int n = 0; n < 4; ++n) { kq[n] = *(LAS f4*)(Ll + L_CONS + (2 * n + hd) * 1024); zq[n] = *(LAS f4*)(L + L_ZC + (2 * n + hd) * 64 + g * 16); }
    SB();
    const f4 k0 = (kq[0] + kq[1]) + (kq[2] + kq[3]), z0 = (zq[0] + zq[1]) + (zq[2] + zq[3]);
    const v2u ka = {pk(k0[0], k0[1]), pk(k0[2], k0[3])}, za = {pk(z0[0], z0[1]), pk(z0[2], z0[3])};
    const f4 n0 = mfma16(ka, pq[0], z4), n1 = mfma16(ka, pq[1], z4), d0 = mfma16(za, pq[0], z4), d1 = mfma16(za, pq[1], z4);
    const float r0 = __builtin_amdgcn_rcpf(d0[0] + 1e-6f), r1 = __builtin_amdgcn_rcpf(d1[0] + 1e-6f);
    const auto sx = __builtin_amdgcn_permlane16_swap(pk(n0[0] * r0, n0[1] * r0), pk(n1[0] * r1, n1[1] * r1), false, false);
    const auto sy = __builtin_amdgcn_permlane16_swap(pk(n0[2] * r0, n0[3] * r0), pk(n1[2] * r1, n1[3] * r1), false, false);
    st16(v4u{sx[0], sy[0], sx[1], sy[1]}, rWS, ((hd ^ g) & 1) * 16384 + ((2 * hd + (g >> 1)) * 16 + i) * 16, WS_O + ohoff - hd * 16384);
  }
  asm volatile("s_waitcnt vmcnt(0)" : "+v"(pf) :: "memory");
  if (!none(pf != tag)) poll(flags + (i * 16 + (j ^ ((g & 1) << 3))) * 32, tag, 32, lane);
  SBAR();
  if (tid == 0) __hip_atomic_store(myflag + 256 * 32, tag, RLX_AGENT);
  STAMP(20);
  {
    const v4u ct = __builtin_amdgcn_raw_buffer_load_b128(rWS, lane * 16, WS_PC + (j * 7 + (w ? w - 1 : 0)) * 1024, SC1);
    const unsigned bias = __builtin_amdgcn_raw_buffer_load_b16(__builtin_amdgcn_make_buffer_rsrc((void*)(b1 + 112 * j), 0, 224, 0x00020000), (tid - 64) * 2, 0, 0);
    if (!(XP & 2)) dmac<4>(dWS, lane * 16, WS_PO + (j * 32 + ((2 * w + rot4) & 15) * 2) * 1024, lds0 + L_WO + w * 4096);
    P1L(4, 2 * w, 0);
    if (w) {
      const f4 c = {rowsum4(dppadd<0x128>(dppadd<0x124>(asf(ct.x)))), rowsum4(dppadd<0x128>(dppadd<0x124>(asf(ct.y)))),
                    rowsum4(dppadd<0x128>(dppadd<0x124>(asf(ct.z)))), rowsum4(dppadd<0x128>(dppadd<0x124>(asf(ct.w))))};
      if (lane < 4) *(LAS f4*)(L + L_C1 + (w - 1) * 64 + lane * 16) = c;
      if (w < 3) *(LAS float*)(L + L_B1 + (tid - 64) * 4) = bflo(bias);
    } else poll(flags + (256 + p * 16 + i) * 32, tag, 16, lane);
  }
  VMWAIT(0);
  SBAR();
  STAMP(8);
#define LOAD1(s) f[s] = __builtin_amdgcn_raw_buffer_load_b128(rWS, lane * 16 + ((s) & 3) * 1024, so + ((((s) & 12) + rot4) & 15) * 1024, SC1)
#define LOADPD() do { _Pragma("unroll") for (int s = 0; s < PD; ++s) { LOAD1(s); SB(); } } while (0)
  {
    const int so = WS_O + (p * 8 + w) * 16384;
    v4u f[16];
    LAS char* const Lo = opq(Ll + L_WO);
    LOADPD();
    f4 a0 = {bflo(xres.x), bfhi(xres.x), bflo(xres.y), bfhi(xres.y)}, a1 = {bflo(xres.z), bfhi(xres.z), bflo(xres.w), bfhi(xres.w)};
    v4u wf2[3][2];
#pragma unroll
    for (int s = 0; s < 2; ++s) { wf2[s][0] = ldf(Lo + s * 2048); wf2[s][1] = ldf(Lo + s * 2048 + 1024); }
#pragma unroll
    for (int s = 0; s < 16; ++s) {
      if (s + PD < 16) LOAD1(s + PD < 16 ? s + PD : 0);
      if (s + 2 < 16) { wf2[(s + 2) % 3][0] = ldf(Lo + (s + 2) * 2048); wf2[(s + 2) % 3][1] = ldf(Lo + (s + 2) * 2048 + 1024); }
      SB();
      a0 = mfma32(wf2[s % 3][0], f[s], a0);
      a1 = mfma32(wf2[s % 3][1], f[s], a1);
      SB();
    }
    P1L(3, 2 * w, 4);
    const v4u h = {pk(a0[0], a0[1]), pk(a0[2], a0[3]), pk(a1[0], a1[1]), pk(a1[2], a1[3])};
    st16(h, rWS, lane * 16, WS_H + ohoff);
    const f4 hs = mfma32(ones, h, z4), hg = mfma32(h, h, z4);
    if (g == q) __builtin_amdgcn_raw_buffer_store_b64(v2u{asu(hs[0]), asu(pp == 0 ? hg[0] : pp == 1 ? hg[1] : pp == 2 ? hg[2] : hg[3])}, rWS, i * 8, WS_S + (ohoff >> 3), SC1);
    if (bid == 0 && tid == 0) __builtin_amdgcn_raw_buffer_store_b32(tag, rWS, WS_EPOCH, 0, SC1);
  }
  STAMP(9);
  VMWAIT(0);
  BAR();
  if (tid == 0) __hip_atomic_store(myflag + 512 * 32, tag, RLX_AGENT);
  STAMP(21);
  v4u w2r[28];
  const int sW2 = WS_P2 + (c4 * 56 + 7 * w) * 4096;
#define W2L(n) w2r[n] = __builtin_amdgcn_raw_buffer_load_b128(rWS, lane * 16 + ((n) & 3) * 1024, sW2 + ((n) >> 2) * 4096, SC1)
  P1L(4, 2 * w + 1, 0); P1L(3, 2 * w + 1, 4);
  if (w == 0) poll(flags + (512 + p * 16 + i) * 32, tag, 16, lane);
  VMWAIT(0);
  SBAR();
  STAMP(10);
  v4u hres;
  constexpr int W2B = 28 - (21 - PD);
  {
    v4u f[16];
    const int so = WS_H + (p * 8 + w) * 16384;
    LAS char* const wp0 = opq(Ll + L_W1);
    LAS char* const wp1 = opq(wp0 + 8 * 7168);
#define W1F(n) ldf(((n) < 56 ? wp0 : wp1) + ((n) % 56) * 1024)
    f4 acc[7];
    LOADPD();
    v2u st[4];
#pragma unroll
    for (int t = 0; t < 7; ++t) acc[t] = z4;
    {
      v4u wf3[6];
#pragma unroll
      for (int n = 0; n < 4; ++n) wf3[n] = W1F(n);
#pragma unroll
      for (int n = 0; n < 112; n += 2) {
        if (n + 4 < 112) { wf3[(n + 4) % 6] = W1F(n + 4); wf3[(n + 5) % 6] = W1F(n + 5); }
        SB();
        acc[(n + 1) % 7] = mfma32(wf3[(n + 1) % 6], f[(n + 1) / 7], acc[(n + 1) % 7]);
        acc[n % 7] = mfma32(wf3[n % 6], f[n / 7], acc[n % 7]);
        const int k = n / 4 + PD;
        if (k < 21) {
          if ((n & 2) == 0) {
            if (k < 16) LOAD1(k < 16 ? k : 0);
            else if (k < 20) st[k & 3] = __builtin_amdgcn_raw_buffer_load_b64(rWS, (4 * g + (k & 3)) * 128 + i * 8, WS_S + (p * 8 + w) * 2048, SC1);
            else hres = __builtin_amdgcn_raw_buffer_load_b128(rWS, (pp * 16 + (lane >> 2)) * 16, WS_H + ((p * 8 + 4 * th + (w >> 1)) * 16 + 2 * c4 + hd) * 1024, SC1);
          }
        } else if ((n & 2) == 0) W2L(k - 21);
        SB();
      }
    }
    f4 cq[7], bq[7];
#pragma unroll
    for (int t = 0; t < 7; ++t) { cq[t] = *(LAS f4*)(L + L_C1 + (t * 16 + 4 * g) * 4); bq[t] = *(LAS f4*)(L + L_B1 + (t * 16 + 4 * g) * 4); }
    const float mean2 = rowsum4((asf(st[0].x) + asf(st[1].x)) + (asf(st[2].x) + asf(st[3].x))) * (1.f / 512.f);
    const float var = fmaxf(rowsum4((asf(st[0].y) + asf(st[1].y)) + (asf(st[2].y) + asf(st[3].y))) * (1.f / 512.f) - mean2 * mean2, 0.f) + 1e-5f;
    const float rstd2 = __builtin_amdgcn_rsqf(var), nm = -mean2 * rstd2;
    STAMP(11);
    v2u e[7];
    const int aoff = WS_A + ((p * 8 + w) * 56 + ((7 * j) >> 1)) * 1024;
#pragma unroll
    for (int t = 0; t < 7; ++t) {
      float y[4];
#pragma unroll
      for (int r = 0; r < 4; ++r) {
        const float v = acc[t][r] * rstd2 + (nm * cq[t][r] + bq[t][r]);
        const float m = __builtin_amdgcn_exp2f(v * LOG2E) + 1.f;
        y[r] = v * (__builtin_amdgcn_rcpf(m * m + 1.f) * -2.f + 1.f);
      }
      e[t] = v2u{pk(y[0], y[1]), pk(y[2], y[3])};
      if (j & 1) {
        if (t == 0) __builtin_amdgcn_raw_buffer_store_b64(e[0], rWS, lane * 16 + 8, aoff, SC1);
        else if ((t & 1) == 0) st16(v4u{e[t - 1].x, e[t - 1].y, e[t].x, e[t].y}, rWS, lane * 16 + (t >> 1) * 1024, aoff);
      } else {
        if (t & 1) st16(v4u{e[t - 1].x, e[t - 1].y, e[t].x, e[t].y}, rWS, lane * 16 + (t >> 1) * 1024, aoff);
        else if (t == 6) __builtin_amdgcn_raw_buffer_store_b64(e[6], rWS, lane * 16 + 3072, aoff, SC1);
      }
    }
  }
  STAMP(13);
#pragma unroll
  for (int n = W2B; n < W2B + W2A; ++n) W2L(n);
  VMWAIT((XP & 1) ? 0 : W2A);
  BAR();
  if (tid == 0) __hip_atomic_store(myflag + 768 * 32, tag, RLX_AGENT);
  STAMP(14);
#pragma unroll
  for (int n = W2B + W2A; n < 28; ++n) W2L(n);
  poll(flags + (768 + p * 16 + 2 * w + (lane & 1)) * 32, tag, 2, lane);
  STAMP(15);
  {
    const int so = WS_A + ((p * 8 + 4 * th) * 56 + 7 * w) * 1024;
    constexpr int NB = PD + 1;
    const int rt = j & 3;
    v4u f[NB];
#define ALOAD(n) __builtin_amdgcn_raw_buffer_load_b128(rWS, lane * 16 + (((n) % 7) & 3) * 1024, so + (((((n) / 7) + rt) & 3) * 56 + (((n) % 7) & 4)) * 1024, SC1)
#pragma unroll
    for (int n = 0; n < NB; ++n) { f[n] = ALOAD(n); SB(); }
    LAS char* const wr = L + (w * 1024 + 16 * g + (i ^ (4 * g))) * 16;
    f4 acc[4];
#pragma unroll
    for (int n = 0; n < 28; ++n) {
      const int s = n % 7, t = n / 7;
#pragma unroll
      for (int u = 0; u < 4; ++u) acc[u] = mfma32(w2r[s * 4 + u], f[n % NB], s ? acc[u] : z4);
      if (n + NB < 28) f[n % NB] = ALOAD(n + NB < 28 ? n + NB : 0);
      if (s == 6) {
#pragma unroll
        for (int u = 0; u < 4; ++u) *(LAS f4*)(wr + ((t + rt) & 3) * 4096 + u * 1024) = acc[u];
      }
      SB();
    }
    STAMP(16);
    BAR();
    STAMP(17);
    LAS char* const rp = opq(L + w * 2048 + (16 * pp + ((lane >> 2) ^ (4 * pp))) * 16);
    LAS char* const rq = opq(rp + 65536);
    f4 t[16];
#pragma unroll
    for (int n = 0; n < 16; ++n) t[n] = *(LAS f4*)((n & 4 ? rq : rp) + (n & 3) * 16384 + (n >> 3) * 1024);
    SB();
    const f4 a0 = ((t[0] + t[1]) + (t[2] + t[3])) + ((t[4] + t[5]) + (t[6] + t[7]));
    const f4 a1 = ((t[8] + t[9]) + (t[10] + t[11])) + ((t[12] + t[13]) + (t[14] + t[15]));
    const v4u o = {pk(a0[0] + bflo(hres.x), a0[1] + bfhi(hres.x)), pk(a0[2] + bflo(hres.y), a0[3] + bfhi(hres.y)),
                   pk(a1[0] + bflo(hres.z), a1[1] + bfhi(hres.z)), pk(a1[2] + bflo(hres.w), a1[3] + bfhi(hres.w))};
    *(v4u*)(out + (size_t)(p * 128 + th * 64 + (w >> 1) * 16 + (lane >> 2)) * 512 + 64 * c4 + 32 * hd + 8 * pp) = o;
  }
  STAMP(18);
  STAMP_FLUSH
}

extern "C" void kernel_launch(void* const* d_in, const int* in_sizes, int n_in, void* d_out, int out_size, void* d_ws, size_t ws_size,
                              hipStream_t stream) {
  (void)in_sizes; (void)n_in; (void)out_size; (void)ws_size;
  (void)hipFuncSetAttribute((const void*)fused_block, hipFuncAttributeMaxDynamicSharedMemorySize, LDS_BYTES);
  fused_block<<<256, 512, LDS_BYTES, stream>>>((const u16*)d_in[0], (const u16*)d_in[1], (const u16*)d_in[2], (const u16*)d_in[4],
                                               (const u16*)d_in[5], (const u16*)d_in[6], (u16*)d_out, (char*)d_ws + ((ws_size - WS_BYTES) & ~(size_t)4095));
}
```

```cpp
#define PA 7
#define PB 8
#define PSEL (1)
#include <hip/hip_runtime.h>
#include <hip/hip_bf16.h>

typedef unsigned short u16;
typedef unsigned v4u __attribute__((ext_vector_type(4)));
typedef unsigned v2u __attribute__((ext_vector_type(2)));
typedef float f4 __attribute__((ext_vector_type(4)));
typedef float f2 __attribute__((ext_vector_type(2)));
typedef short s4 __attribute__((ext_vector_type(4)));
typedef __bf16 b8 __attribute__((ext_vector_type(8)));
typedef __bf16 b2 __attribute__((ext_vector_type(2)));
#define GAS __attribute__((address_space(1)))
#define LAS __attribute__((address_space(3)))
#if defined(__HIP_DEVICE_COMPILE__)
#define TGT __attribute__((target("no-packed-fp32-ops")))
#else
#define TGT
#endif
#define DI __device__ __forceinline__ TGT
#define RLX_AGENT __ATOMIC_RELAXED, __HIP_MEMORY_SCOPE_AGENT

#define MAPMODE 1
#define XP 0
#define PD 6
#define KVAT 12
#define W2A 4

constexpr int WS_EPOCH = 0;
constexpr int WS_FLAG = 4096;
constexpr int WS_XS = 196608;
constexpr int WS_KV = 262144;
constexpr int KV_REC = 1152;
constexpr int WS_O = 1 << 20;
constexpr int WS_H = 3 << 20;
constexpr int WS_A = 5 << 20;
constexpr int WS_S = 12 << 20;
constexpr int WS_P1 = 13 << 20;
constexpr int WS_PO = 15 << 20;
constexpr int WS_PC = WS_PO + (1 << 19);
constexpr int WS_P2 = 16 << 20;
constexpr int WS_BYTES = 18 << 20;
#define STAMP(n) do { if ((n) == PA) asm volatile("s_memrealtime %0\n\ts_waitcnt lgkmcnt(0)" : "=s"(ta_) :: "memory"); if ((n) == PB) asm volatile("s_memrealtime %0\n\ts_waitcnt lgkmcnt(0)" : "=s"(tb_) :: "memory"); } while (0)
#define STAMP_DECL unsigned long long ta_ = 0, tb_ = 0;
#define STAMP_FLUSH if (w == 4 && PSEL) { const unsigned long long te_ = __builtin_amdgcn_s_memrealtime() + 4 * ((tb_ - ta_) & 0xfff); while (__builtin_amdgcn_s_memrealtime() < te_) __builtin_amdgcn_s_sleep(1); }
#define COUNT(n)

constexpr int L_WQ = 0, L_WK = 32768, L_KVRED = 98304, L_WO = 114688, L_SCR = 147456;
constexpr int L_RAW = 0;
constexpr int L_W1 = 0;
constexpr int L_CONS = L_SCR;
constexpr int L_ZP = L_SCR + 8192;
constexpr int L_ZC = L_ZP + 512;
constexpr int L_CKV = L_ZC + 512;
constexpr int L_STT = L_CKV + 512;
constexpr int L_C1 = L_STT + 1024;
constexpr int L_B1 = L_C1 + 512;
constexpr int LDS_BYTES = 163840;

constexpr unsigned ONE2 = 0x3F803F80u;
constexpr int SC1 = 16;
constexpr float LOG2E = 1.4426950408889634f;
constexpr unsigned SPIN = 1u << 15;

DI float asf(unsigned u) { return __builtin_bit_cast(float, u); }
DI unsigned asu(float f) { return __builtin_bit_cast(unsigned, f); }
DI f4 mfma32(v4u a, v4u b, f4 c) { return __builtin_amdgcn_mfma_f32_16x16x32_bf16(__builtin_bit_cast(b8, a), __builtin_bit_cast(b8, b), c, 0, 0, 0); }
DI f4 mfma16(v2u a, v2u b, f4 c) { return __builtin_amdgcn_mfma_f32_16x16x16bf16_1k(__builtin_bit_cast(s4, a), __builtin_bit_cast(s4, b), c, 0, 0, 0); }
DI unsigned pk(float lo, float hi) { f2 v = {lo, hi}; return __builtin_bit_cast(unsigned, __builtin_convertvector(v, b2)); }
DI float bflo(unsigned u) { return asf(u << 16); }
DI float bfhi(unsigned u) { return asf(u & 0xffff0000u); }
DI v2u trd(LAS char* p) { return __builtin_bit_cast(v2u, __builtin_amdgcn_ds_read_tr16_b64_v4i16((LAS s4*)p)); }
DI v4u trfrag(LAS char* p, int second) { v2u a = trd(p), b = trd(p + second); return v4u{a.x, a.y, b.x, b.y}; }
DI v4u ldf(LAS char* p) { return *(LAS v4u*)p; }
DI float dot2(unsigned a, unsigned b, float c) { return __builtin_amdgcn_fdot2_f32_bf16(__builtin_bit_cast(b2, a), __builtin_bit_cast(b2, b), c, false); }
template <int C> DI float dppadd(float v) { return v + asf(__builtin_amdgcn_mov_dpp(asu(v), C, 0xf, 0xf, true)); }
DI float rowsum4(float v) {
  auto r = __builtin_amdgcn_permlane32_swap(asu(v), asu(v), false, false);
  const float s = asf(r[0]) + asf(r[1]);
  auto t = __builtin_amdgcn_permlane16_swap(asu(s), asu(s), false, false);
  return asf(t[0]) + asf(t[1]);
}
DI bool none(bool c) { return __builtin_amdgcn_ballot_w64(c) == 0; }
DI v4u mksrd(const void* p, unsigned bytes) {
  unsigned long long a = (unsigned long long)p;
  return v4u{(unsigned)a, (unsigned)(a >> 32) & 0xffffu, bytes, 0x00020000u};
}
DI void dma16(v4u srd, unsigned voff, unsigned soff, unsigned lds) {
  asm volatile("s_mov_b32 m0, %3\n\ts_nop 0\n\tbuffer_load_dwordx4 %0, %1, %2 offen lds" ::"v"(voff), "s"(srd), "s"(soff), "s"(lds) : "memory");
}
template <int N> DI void dmac(v4u srd, unsigned voff, unsigned so, unsigned lds) {
  if (N == 1) asm volatile("s_mov_b32 m0, %3\n\ts_nop 0\n\tbuffer_load_dwordx4 %0, %1, %2 offen sc1 lds" ::"v"(voff), "s"(srd), "s"(so), "s"(lds) : "memory");
  if (N == 3) asm volatile("s_mov_b32 m0, %3\n\ts_nop 0\n\tbuffer_load_dwordx4 %0, %1, %2 offen sc1 lds\n\tbuffer_load_dwordx4 %0, %1, %2 offen offset:1024 sc1 lds\n\t"
                           "buffer_load_dwordx4 %0, %1, %2 offen offset:2048 sc1 lds" ::"v"(voff), "s"(srd), "s"(so), "s"(lds) : "memory");
  if (N == 4) asm volatile("s_mov_b32 m0, %3\n\ts_nop 0\n\tbuffer_load_dwordx4 %0, %1, %2 offen sc1 lds\n\tbuffer_load_dwordx4 %0, %1, %2 offen offset:1024 sc1 lds\n\t"
                           "buffer_load_dwordx4 %0, %1, %2 offen offset:2048 sc1 lds\n\tbuffer_load_dwordx4 %0, %1, %2 offen offset:3072 sc1 lds"
                           ::"v"(voff), "s"(srd), "s"(so), "s"(lds) : "memory");
}
#define LD16(dst, srd, voff, soff) asm volatile("buffer_load_dwordx4 %0, %1, %2, %3 offen sc1" : "=v"(dst) : "v"(voff), "s"(srd), "s"(soff) : "memory")
DI void st16(v4u v, __amdgpu_buffer_rsrc_t r, int voff, int soff) {
  __builtin_amdgcn_raw_buffer_store_b128(v, r, voff, soff, SC1);
  asm volatile("s_nop 1" ::"v"(v) : "memory");
}
DI LAS char* opq(LAS char* p) { asm volatile("" : "+v"(p)); return p; }
#define LGKM0() asm volatile("s_waitcnt lgkmcnt(0)" ::: "memory")
#define SBAR() do { __builtin_amdgcn_s_barrier(); asm volatile("" ::: "memory"); } while (0)
#define BAR() do { LGKM0(); SBAR(); } while (0)
#define VMWAIT(n) asm volatile("s_waitcnt vmcnt(%0)" ::"i"(n) : "memory")
#define SB() __builtin_amdgcn_sched_barrier(0)

DI float phi(float t) { return fmaxf(t, 0.f) + __builtin_amdgcn_exp2f(fminf(t, 0.f) * LOG2E) + 1e-6f; }
DI void poll(GAS unsigned* fp, unsigned tag, int n, int lane) {
#pragma nounroll
  for (unsigned sp = 0; sp < SPIN; ++sp) {
    const unsigned v = lane < n ? __hip_atomic_load(fp, RLX_AGENT) : tag;
    if (none(v != tag)) break;
  }
  asm volatile("" ::: "memory");
}

__global__ void __launch_bounds__(512, 2) TGT
fused_block(const u16* __restrict__ x, const u16* __restrict__ wqkv, const u16* __restrict__ wo, const u16* __restrict__ w1,
            const u16* __restrict__ b1, const u16* __restrict__ w2, u16* __restrict__ out, char* ws) {
  extern __shared__ __attribute__((aligned(16))) char smem_[];
  LAS char* L = (LAS char*)smem_;
  const int tid = threadIdx.x, lane = tid & 63, w = __builtin_amdgcn_readfirstlane(tid >> 6);
  const int g = lane >> 4, i = lane & 15, q = (lane >> 2) & 3, pp = lane & 3;
  const int bid = blockIdx.x, xcd = bid & 7, rk = bid >> 3;
  const int p = 4 * (xcd >> 1) + (rk >> 3), j = (xcd & 1) * 8 + (rk & 7);
  const int rot = 2 * p + j;
  STAMP_DECL
  STAMP(0);
  const v4u dQKV = mksrd(wqkv, 512 * 1536 * 2), dX = mksrd(x, 2048 * 512 * 2), dWS = mksrd(ws, WS_BYTES);
  const __amdgpu_buffer_rsrc_t rWS = __builtin_amdgcn_make_buffer_rsrc(ws, 0, WS_BYTES, 0x00020000);
  const unsigned lds0 = (unsigned)(size_t)L;
  LAS char* const Ll = L + lane * 16;
  const int th = j >> 3, c4 = j & 7;

  const unsigned vW = (lane >> 2) * 3072 + (pp ^ ((lane >> 5) << 1)) * 16;
  const unsigned vX0 = (lane >> 3) * 1024 + ((lane & 7) ^ g) * 16, vX1 = (vX0 ^ 64) + 8192;
  const unsigned sX = (p * 128 + w * 16) * 1024;
  const unsigned sW = (w & 3) * (16 * 3072) + (1 + (w >> 2)) * 1024 + j * 64, lW = lds0 + L_WK + (w >> 2) * 32768 + (w & 3) * 1024;
#define XSLOT(c) ((c) % 3 == 0 ? L_KVRED + w * 2048 : L_WO + w * 4096 + ((c) % 3 - 1) * 2048)
#define WLOAD(c) dma16(dQKV, vW, sW + (((c) + rot) & 7) * (64 * 3072), lW + (c) * 4096)
#define XLOAD1(c, h) dma16(dX, (h) ? vX1 : vX0, sX + (((c) + rot) & 7) * 128, lds0 + XSLOT(c) + (h) * 1024)
#define QLOAD1(n) dma16(dQKV, vW, (((w + rot) & 7) * 64 + (n) * 16) * 3072 + j * 64, lds0 + L_WQ + w * 4096 + (n) * 1024)
  unsigned tag;
  v4u xrow;
  asm volatile("buffer_load_dwordx4 %0, %1, %2, %3 offen" : "=v"(xrow) : "v"(lane * 16), "s"(dX), "s"((p * 128 + j * 8 + w) * 1024) : "memory");
  asm volatile("buffer_load_dword %0, off, %1, 0 sc1" : "=v"(tag) : "s"(dWS) : "memory");
  WLOAD(0); XLOAD1(0, 0); XLOAD1(0, 1);
  SBAR();
  WLOAD(1); XLOAD1(1, 0); XLOAD1(1, 1);
  const int u2 = 2 * p + th + 32 * (w >> 2);
  {
    const unsigned c = w * 64 + lane, r = (c * 4682u) >> 16;
    if (w < 7) dma16(mksrd(w1, 512 * 1792 * 2), r * 3584 + (c - r * 14) * 16, p * (32 * 3584) + 224 * j, lds0 + L_RAW + w * 1024);
    if (w >= 6) dma16(mksrd(wo, 512 * 512 * 2), (lane >> 2) * 1024 + pp * 16, (p * 32 + (w & 1) * 16) * 1024 + j * 64, lds0 + L_RAW + 7168 + (w & 1) * 1024);
    dma16(mksrd(w2, 1792 * 512 * 2), (lane >> 3) * 1024 + (lane & 7) * 16, (u2 * 32 + (w & 3) * 8) * 1024 + c4 * 128, lds0 + L_RAW + 9216 + w * 1024);
  }
  STAMP(1);
  asm volatile("s_waitcnt vmcnt(8)" : "+v"(xrow), "+v"(tag) :: "memory");
  tag += 1u;
  {
    float s = dot2(xrow.x, ONE2, dot2(xrow.y, ONE2, 0.f)) + dot2(xrow.z, ONE2, dot2(xrow.w, ONE2, 0.f));
    float ss = dot2(xrow.x, xrow.x, dot2(xrow.y, xrow.y, 0.f)) + dot2(xrow.z, xrow.z, dot2(xrow.w, xrow.w, 0.f));
    s = rowsum4(dppadd<0x128>(dppadd<0x124>(dppadd<0x122>(dppadd<0x121>(s))))) * (1.f / 512.f);
    ss = rowsum4(dppadd<0x128>(dppadd<0x124>(dppadd<0x122>(dppadd<0x121>(ss))))) * (1.f / 512.f);
    const float r = __builtin_amdgcn_rsqf(fmaxf(ss - s * s, 0.f) + 1e-5f);
    if (lane == 0) st16(v4u{asu(s), tag, asu(r), tag}, rWS, 0, WS_XS + (p * 128 + j * 8 + w) * 16);
  }
  GAS unsigned* const flags = (GAS unsigned*)(ws + WS_FLAG);
  GAS unsigned* const myflag = flags + (p * 16 + j) * 32;
  const int hd = w & 1;
  LAS char* const tN = L + (8 * g + q) * 64 + pp * 8 + ((g ^ hd) & 1) * 32;
  LAS char* const kN = opq(tN + L_WK);
  LAS char* const kC = opq(tN + L_WK + ((w >> 1) & 1) * 32768 + (w >> 2) * 2048);
#define WFRAG(s, m) trfrag(kN + (m) * 32768 + (s) * 2048, 256)
  v4u ones = {ONE2, ONE2, ONE2, ONE2};
  asm volatile("" : "+v"(ones));
  v4u xf[2][16], xres;
  const f4 z4 = {0, 0, 0, 0};
  f4 ak[2] = {z4, z4}, av[2] = {z4, z4}, cs = z4;
  const int xrd = i * 128 + (g ^ (i >> 1)) * 16;
  LAS char* xp[2][2][2];
#pragma unroll
  for (int o = 0; o < 2; ++o)
#pragma unroll
    for (int h = 0; h < 2; ++h) { xp[o][0][h] = L + L_KVRED + (w ^ o) * 2048 + (xrd ^ (h * 64)); xp[o][1][h] = L + L_WO + (w ^ o) * 4096 + (xrd ^ (h * 64)); }
#define XRD(o, c, h) xf[o][2 * (c) + (h)] = ldf((c) % 3 ? xp[o][1][h] + ((c) % 3 - 1) * 2048 : xp[o][0][h])
  v4u wf[2][4], cf, xs, pr1, pro, pr2;
  f4 prc;
  f4 mr[2], rs[2];
  f2 c2k, c2v;
  const int cres = ((j >> 1) - rot) & 7;
  const int rot4 = (j & 3) * 4;
#define P1L(N, slot, n) if (!(XP & 2)) dmac<N>(dWS, lane * 16, WS_P1 + (j * 112 + (((slot) + rot4) & 15) * 7 + (n)) * 1024, lds0 + L_W1 + ((slot) * 7 + (n)) * 1024)
#pragma unroll
  for (int c = -1; c < 8; ++c) {
    if (c >= 0) {
      ak[0] = mfma32(xf[0][2 * c], wf[c & 1][0], ak[0]);
      ak[1] = mfma32(xf[1][2 * c], wf[c & 1][0], ak[1]);
      SB();
    }
    if (c < 6) VMWAIT(c < 1 ? 5 : 3);
    else if (c == 6) {
      asm volatile("s_waitcnt vmcnt(2)" : "+v"(xs) :: "memory");
#pragma nounroll
      for (unsigned sp = 0; sp < SPIN && !none(xs.y != tag || xs.w != tag); ++sp) {
        COUNT(23);
        xs = __builtin_amdgcn_raw_buffer_load_b128(rWS, i * 16, WS_XS + (p * 128 + w * 16) * 16, SC1);
        asm volatile("" ::: "memory");
      }
      if (g == 0) { *(LAS unsigned*)(L + L_STT + w * 128 + i * 4) = xs.x; *(LAS unsigned*)(L + L_STT + w * 128 + 64 + i * 4) = xs.z; }
    } else LGKM0();
    SBAR();
    if (c >= 0) {
      av[0] = mfma32(xf[0][2 * c], wf[c & 1][1], av[0]);
      av[1] = mfma32(xf[1][2 * c], wf[c & 1][1], av[1]);
    }
    if (c < 7) {
      XRD(0, c + 1, 0); XRD(1, c + 1, 0); XRD(0, c + 1, 1); XRD(1, c + 1, 1);
      cf = trfrag(kC + (c + 1) * 4096, 256);
    } else {
#pragma unroll
      for (int o = 0; o < 2; ++o) { mr[o] = *(LAS f4*)(L + L_STT + (w ^ o) * 128 + g * 16); rs[o] = *(LAS f4*)(L + L_STT + (w ^ o) * 128 + 64 + g * 16); }
      c2k = *(LAS f2*)(L + L_CKV + (hd * 16 + i) * 8); c2v = *(LAS f2*)(L + L_CKV + (32 + hd * 16 + i) * 8);
    }
    if (c < 5) WLOAD(c + 3);
    if (c == 5) LD16(xs, dWS, i * 16, WS_XS + (p * 128 + w * 16) * 16);
    SB();
    if (c >= 0) {
      ak[0] = mfma32(xf[0][2 * c + 1], wf[c & 1][2], ak[0]);
      ak[1] = mfma32(xf[1][2 * c + 1], wf[c & 1][2], ak[1]);
    }
    if (c < 7) { wf[(c + 1) & 1][0] = WFRAG(2 * c + 2, 0); wf[(c + 1) & 1][1] = WFRAG(2 * c + 2, 1); }
    if (c < 5) XLOAD1(c + 3, 0); else if (c < 7) QLOAD1(2 * c - 10);
    SB();
    if (c >= 0) {
      av[0] = mfma32(xf[0][2 * c + 1], wf[c & 1][3], av[0]);
      av[1] = mfma32(xf[1][2 * c + 1], wf[c & 1][3], av[1]);
    }
    if (c < 7) { wf[(c + 1) & 1][2] = WFRAG(2 * c + 3, 0); wf[(c + 1) & 1][3] = WFRAG(2 * c + 3, 1); }
    if (c < 5) XLOAD1(c + 3, 1); else if (c < 7) QLOAD1(2 * c - 9);
    if (c == 7) {
      st16(pr1, rWS, lane * 16, w < 7 ? WS_P1 + ((j * 16 + p) * 7 + w) * 1024 : WS_BYTES);
      st16(pr2, rWS, lane * 16, u2 < 56 ? WS_P2 + ((c4 * 56 + u2) * 4 + (w & 3)) * 1024 : WS_BYTES);
      if (i == 0) st16(__builtin_bit_cast(v4u, prc), rWS, g * 16, w < 7 ? WS_PC + ((j * 7 + w) * 16 + p) * 64 : WS_BYTES);
      if (w >= 6) st16(pro, rWS, lane * 16, WS_PO + ((j * 16 + p) * 2 + (w & 1)) * 1024);
    }
    SB();
    if (c < 7) {
      cs = mfma32(ones, cf, cs);
      if (c + 1 == cres) xres = (j & 1) ? xf[0][2 * c + 3] : xf[0][2 * c + 2];
    }
    if (c == 1) {
      const int il = (pp * 2 + (w & 1)) * 8;
      pr1 = trfrag(L + L_RAW + (8 * g + q) * 224 + pp * 8 + w * 32, 4 * 224);
      pro = trfrag(L + L_RAW + 7168 + (8 * g + q) * 64 + il, 256);
      pr2 = trfrag(L + L_RAW + 9216 + (w >> 2) * 4096 + (4 * g + q) * 128 + (w & 2) * 32 + il, 2048);
    }
    if (c == 2) prc = mfma32(pr1, ones, z4);
    if (c == 6 && g == 0) *(LAS float*)(L + L_CKV + ((w & 3) * 16 + i) * 8 + (w >> 2) * 4) = cs[0];
  }
  STAMP(3);
  {
    const float ck = c2k[0] + c2k[1], cv = c2v[0] + c2v[1];
    float kk[8], vv[8];
#pragma unroll
    for (int n = 0; n < 8; ++n) {
      kk[n] = phi((ak[n >> 2][n & 3] - mr[n >> 2][n & 3] * ck) * rs[n >> 2][n & 3]);
      vv[n] = (av[n >> 2][n & 3] - mr[n >> 2][n & 3] * cv) * rs[n >> 2][n & 3];
    }
    const v4u pkf = {pk(kk[0], kk[1]), pk(kk[2], kk[3]), pk(kk[4], kk[5]), pk(kk[6], kk[7])};
    const v4u pvf = {pk(vv[0], vv[1]), pk(vv[2], vv[3]), pk(vv[4], vv[5]), pk(vv[6], vv[7])};
    const f4 kv = mfma32(pkf, pvf, z4), z = mfma32(pkf, ones, z4);
    *(LAS f4*)(Ll + L_KVRED + w * 1024) = kv;
    if (i == 0) *(LAS f4*)(L + L_ZP + w * 64 + g * 16) = z;
  }
  if (w < 6) VMWAIT(3); else VMWAIT(4);
  BAR();
  STAMP(4);
  if (w < 2) {
    __builtin_amdgcn_s_setprio(3);
    f4 t[8];
#pragma unroll
    for (int n = 0; n < 4; ++n) { t[n] = *(LAS f4*)(Ll + L_KVRED + (n * 2 + w) * 1024); t[4 + n] = *(LAS f4*)(L + L_ZP + (n * 2 + w) * 64 + pp * 16); }
    SB();
    const int so = WS_KV + ((j * 2 + w) * 16 + p) * KV_REC;
    const f4 a = (t[0] + t[1]) + (t[2] + t[3]), z = (t[4] + t[5]) + (t[6] + t[7]);
    st16(v4u{pk(a[0], a[1]), tag, pk(a[2], a[3]), tag}, rWS, lane * 16, so);
    if (lane < 4) st16(v4u{pk(z[0], z[1]), tag, pk(z[2], z[3]), tag}, rWS, lane * 16 + 1024, so);
    __builtin_amdgcn_s_setprio(0);
    STAMP(2);
  }
  v2u pq[2];
  v4u kt[5];
  const unsigned kso = WS_KV + ((j * 2 + hd) * 16 + 4 * (w >> 1)) * KV_REC;
  {
    f4 aq[2] = {z4, z4}, cq = z4;
    v4u qf[4];
#pragma unroll
    for (int s = 0; s < 3; ++s) qf[s] = trfrag(tN + L_WQ + s * 2048, 256);
#pragma unroll
    for (int s = 0; s < 16; ++s) {
      if (s + 3 < 16) qf[(s + 3) & 3] = trfrag(tN + L_WQ + (s + 3) * 2048, 256);
      aq[0] = mfma32(qf[s & 3], xf[0][s], aq[0]);
      aq[1] = mfma32(qf[s & 3], xf[1][s], aq[1]);
      cq = mfma32(qf[s & 3], ones, cq);
      if (s == 8) {
        if (w < 2) VMWAIT(2); else VMWAIT(0);
        SBAR();
        if (tid == 128) __hip_atomic_store(myflag, tag, RLX_AGENT);
      }
      if (s == KVAT) {
#pragma unroll
        for (int n = 0; n < 4; ++n) LD16(kt[n], dWS, lane * 16, kso + n * KV_REC);
        LD16(kt[4], dWS, q * KV_REC + pp * 16 + 1024, kso);
      }
      SB();
    }
#pragma unroll
    for (int o = 0; o < 2; ++o) {
      const float m = *(LAS float*)(L + L_STT + (w ^ o) * 128 + i * 4), r = *(LAS float*)(L + L_STT + (w ^ o) * 128 + 64 + i * 4);
      float a[4];
#pragma unroll
      for (int n = 0; n < 4; ++n) a[n] = phi((aq[o][n] - m * cq[n]) * r);
      pq[o] = v2u{pk(a[0], a[1]), pk(a[2], a[3])};
    }
    asm volatile("" : "+v"(pq[0]), "+v"(pq[1]));
  }
  STAMP(5);
  {
    asm volatile("s_waitcnt vmcnt(0)" : "+v"(kt[0]), "+v"(kt[1]), "+v"(kt[2]), "+v"(kt[3]), "+v"(kt[4]) :: "memory");
#pragma nounroll
    for (unsigned sp = 0; sp < SPIN; ++sp) {
      unsigned bad = 0;
#pragma unroll
      for (int n = 0; n < 5; ++n) bad |= (kt[n].y ^ tag) | (kt[n].w ^ tag);
      if (none(bad != 0)) break;
      COUNT(24);
#pragma unroll
      for (int n = 0; n < 4; ++n) kt[n] = __builtin_amdgcn_raw_buffer_load_b128(rWS, lane * 16, kso + n * KV_REC, SC1);
      kt[4] = __builtin_amdgcn_raw_buffer_load_b128(rWS, q * KV_REC + pp * 16 + 1024, kso, SC1);
      asm volatile("" ::: "memory");
    }
    STAMP(6);
    const f4 s = {(bflo(kt[0].x) + bflo(kt[1].x)) + (bflo(kt[2].x) + bflo(kt[3].x)), (bfhi(kt[0].x) + bfhi(kt[1].x)) + (bfhi(kt[2].x) + bfhi(kt[3].x)),
                  (bflo(kt[0].z) + bflo(kt[1].z)) + (bflo(kt[2].z) + bflo(kt[3].z)), (bfhi(kt[0].z) + bfhi(kt[1].z)) + (bfhi(kt[2].z) + bfhi(kt[3].z))};
    *(LAS f4*)(Ll + L_CONS + w * 1024) = s;
    const f4 z = {dppadd<0x128>(dppadd<0x124>(bflo(kt[4].x))), dppadd<0x128>(dppadd<0x124>(bfhi(kt[4].x))),
                  dppadd<0x128>(dppadd<0x124>(bflo(kt[4].z))), dppadd<0x128>(dppadd<0x124>(bfhi(kt[4].z)))};
    if (lane < 4) *(LAS f4*)(L + L_ZC + w * 64 + lane * 16) = z;
  }
  BAR();
  STAMP(7);
  unsigned pf = tag;
  if (w == 0) asm volatile("buffer_load_dword %0, %1, %2, %3 offen sc1" : "+v"(pf) : "v"((i * 16 + (j ^ ((g & 1) << 3))) * 128), "s"(dWS), "s"(WS_FLAG) : "memory");
  const int ohoff = ((p * 8 + w) * 16 + j) * 1024;
  {
    f4 kq[4], zq[4];
#pragma unroll
    for (int n = 0; n < 4; ++n) { kq[n] = *(LAS f4*)(Ll + L_CONS + (2 * n + hd) * 1024); zq[n] = *(LAS f4*)(L + L_ZC + (2 * n + hd) * 64 + g * 16); }
    SB();
    const f4 k0 = (kq[0] + kq[1]) + (kq[2] + kq[3]), z0 = (zq[0] + zq[1]) + (zq[2] + zq[3]);
    const v2u ka = {pk(k0[0], k0[1]), pk(k0[2], k0[3])}, za = {pk(z0[0], z0[1]), pk(z0[2], z0[3])};
    const f4 n0 = mfma16(ka, pq[0], z4), n1 = mfma16(ka, pq[1], z4), d0 = mfma16(za, pq[0], z4), d1 = mfma16(za, pq[1], z4);
    const float r0 = __builtin_amdgcn_rcpf(d0[0] + 1e-6f), r1 = __builtin_amdgcn_rcpf(d1[0] + 1e-6f);
    const auto sx = __builtin_amdgcn_permlane16_swap(pk(n0[0] * r0, n0[1] * r0), pk(n1[0] * r1, n1[1] * r1), false, false);
    const auto sy = __builtin_amdgcn_permlane16_swap(pk(n0[2] * r0, n0[3] * r0), pk(n1[2] * r1, n1[3] * r1), false, false);
    st16(v4u{sx[0], sy[0], sx[1], sy[1]}, rWS, ((hd ^ g) & 1) * 16384 + ((2 * hd + (g >> 1)) * 16 + i) * 16, WS_O + ohoff - hd * 16384);
  }
  asm volatile("s_waitcnt vmcnt(0)" : "+v"(pf) :: "memory");
  if (!none(pf != tag)) poll(flags + (i * 16 + (j ^ ((g & 1) << 3))) * 32, tag, 32, lane);
  SBAR();
  if (tid == 0) __hip_atomic_store(myflag + 256 * 32, tag, RLX_AGENT);
  STAMP(20);
  {
    const v4u ct = __builtin_amdgcn_raw_buffer_load_b128(rWS, lane * 16, WS_PC + (j * 7 + (w ? w - 1 : 0)) * 1024, SC1);
    const unsigned bias = __builtin_amdgcn_raw_buffer_load_b16(__builtin_amdgcn_make_buffer_rsrc((void*)(b1 + 112 * j), 0, 224, 0x00020000), (tid - 64) * 2, 0, 0);
    if (!(XP & 2)) dmac<4>(dWS, lane * 16, WS_PO + (j * 32 + ((2 * w + rot4) & 15) * 2) * 1024, lds0 + L_WO + w * 4096);
    P1L(4, 2 * w, 0);
    if (w) {
      const f4 c = {rowsum4(dppadd<0x128>(dppadd<0x124>(asf(ct.x)))), rowsum4(dppadd<0x128>(dppadd<0x124>(asf(ct.y)))),
                    rowsum4(dppadd<0x128>(dppadd<0x124>(asf(ct.z)))), rowsum4(dppadd<0x128>(dppadd<0x124>(asf(ct.w))))};
      if (lane < 4) *(LAS f4*)(L + L_C1 + (w - 1) * 64 + lane * 16) = c;
      if (w < 3) *(LAS float*)(L + L_B1 + (tid - 64) * 4) = bflo(bias);
    } else poll(flags + (256 + p * 16 + i) * 32, tag, 16, lane);
  }
  VMWAIT(0);
  SBAR();
  STAMP(8);
#define LOAD1(s) f[s] = __builtin_amdgcn_raw_buffer_load_b128(rWS, lane * 16 + ((s) & 3) * 1024, so + ((((s) & 12) + rot4) & 15) * 1024, SC1)
#define LOADPD() do { _Pragma("unroll") for (int s = 0; s < PD; ++s) { LOAD1(s); SB(); } } while (0)
  {
    const int so = WS_O + (p * 8 + w) * 16384;
    v4u f[16];
    LAS char* const Lo = opq(Ll + L_WO);
    LOADPD();
    f4 a0 = {bflo(xres.x), bfhi(xres.x), bflo(xres.y), bfhi(xres.y)}, a1 = {bflo(xres.z), bfhi(xres.z), bflo(xres.w), bfhi(xres.w)};
    v4u wf2[3][2];
#pragma unroll
    for (int s = 0; s < 2; ++s) { wf2[s][0] = ldf(Lo + s * 2048); wf2[s][1] = ldf(Lo + s * 2048 + 1024); }
#pragma unroll
    for (int s = 0; s < 16; ++s) {
      if (s + PD < 16) LOAD1(s + PD < 16 ? s + PD : 0);
      if (s + 2 < 16) { wf2[(s + 2) % 3][0] = ldf(Lo + (s + 2) * 2048); wf2[(s + 2) % 3][1] = ldf(Lo + (s + 2) * 2048 + 1024); }
      SB();
      a0 = mfma32(wf2[s % 3][0], f[s], a0);
      a1 = mfma32(wf2[s % 3][1], f[s], a1);
      SB();
    }
    P1L(3, 2 * w, 4);
    const v4u h = {pk(a0[0], a0[1]), pk(a0[2], a0[3]), pk(a1[0], a1[1]), pk(a1[2], a1[3])};
    st16(h, rWS, lane * 16, WS_H + ohoff);
    const f4 hs = mfma32(ones, h, z4), hg = mfma32(h, h, z4);
    if (g == q) __builtin_amdgcn_raw_buffer_store_b64(v2u{asu(hs[0]), asu(pp == 0 ? hg[0] : pp == 1 ? hg[1] : pp == 2 ? hg[2] : hg[3])}, rWS, i * 8, WS_S + (ohoff >> 3), SC1);
    if (bid == 0 && tid == 0) __builtin_amdgcn_raw_buffer_store_b32(tag, rWS, WS_EPOCH, 0, SC1);
  }
  STAMP(9);
  VMWAIT(0);
  BAR();
  if (tid == 0) __hip_atomic_store(myflag + 512 * 32, tag, RLX_AGENT);
  STAMP(21);
  v4u w2r[28];
  const int sW2 = WS_P2 + (c4 * 56 + 7 * w) * 4096;
#define W2L(n) w2r[n] = __builtin_amdgcn_raw_buffer_load_b128(rWS, lane * 16 + ((n) & 3) * 1024, sW2 + ((n) >> 2) * 4096, SC1)
  P1L(4, 2 * w + 1, 0); P1L(3, 2 * w + 1, 4);
  if (w == 0) poll(flags + (512 + p * 16 + i) * 32, tag, 16, lane);
  VMWAIT(0);
  SBAR();
  STAMP(10);
  v4u hres;
  constexpr int W2B = 28 - (21 - PD);
  {
    v4u f[16];
    const int so = WS_H + (p * 8 + w) * 16384;
    LAS char* const wp0 = opq(Ll + L_W1);
    LAS char* const wp1 = opq(wp0 + 8 * 7168);
#define W1F(n) ldf(((n) < 56 ? wp0 : wp1) + ((n) % 56) * 1024)
    f4 acc[7];
    LOADPD();
    v2u st[4];
#pragma unroll
    for (int t = 0; t < 7; ++t) acc[t] = z4;
    {
      v4u wf3[6];
#pragma unroll
      for (int n = 0; n < 4; ++n) wf3[n] = W1F(n);
#pragma unroll
      for (int n = 0; n < 112; n += 2) {
        if (n + 4 < 112) { wf3[(n + 4) % 6] = W1F(n + 4); wf3[(n + 5) % 6] = W1F(n + 5); }
        SB();
        acc[(n + 1) % 7] = mfma32(wf3[(n + 1) % 6], f[(n + 1) / 7], acc[(n + 1) % 7]);
        acc[n % 7] = mfma32(wf3[n % 6], f[n / 7], acc[n % 7]);
        const int k = n / 4 + PD;
        if (k < 21) {
          if ((n & 2) == 0) {
            if (k < 16) LOAD1(k < 16 ? k : 0);
            else if (k < 20) st[k & 3] = __builtin_amdgcn_raw_buffer_load_b64(rWS, (4 * g + (k & 3)) * 128 + i * 8, WS_S + (p * 8 + w) * 2048, SC1);
            else hres = __builtin_amdgcn_raw_buffer_load_b128(rWS, (pp * 16 + (lane >> 2)) * 16, WS_H + ((p * 8 + 4 * th + (w >> 1)) * 16 + 2 * c4 + hd) * 1024, SC1);
          }
        } else if ((n & 2) == 0) W2L(k - 21);
        SB();
      }
    }
    f4 cq[7], bq[7];
#pragma unroll
    for (int t = 0; t < 7; ++t) { cq[t] = *(LAS f4*)(L + L_C1 + (t * 16 + 4 * g) * 4); bq[t] = *(LAS f4*)(L + L_B1 + (t * 16 + 4 * g) * 4); }
    const float mean2 = rowsum4((asf(st[0].x) + asf(st[1].x)) + (asf(st[2].x) + asf(st[3].x))) * (1.f / 512.f);
    const float var = fmaxf(rowsum4((asf(st[0].y) + asf(st[1].y)) + (asf(st[2].y) + asf(st[3].y))) * (1.f / 512.f) - mean2 * mean2, 0.f) + 1e-5f;
    const float rstd2 = __builtin_amdgcn_rsqf(var), nm = -mean2 * rstd2;
    STAMP(11);
    v2u e[7];
    const int aoff = WS_A + ((p * 8 + w) * 56 + ((7 * j) >> 1)) * 1024;
#pragma unroll
    for (int t = 0; t < 7; ++t) {
      float y[4];
#pragma unroll
      for (int r = 0; r < 4; ++r) {
        const float v = acc[t][r] * rstd2 + (nm * cq[t][r] + bq[t][r]);
        const float m = __builtin_amdgcn_exp2f(v * LOG2E) + 1.f;
        y[r] = v * (__builtin_amdgcn_rcpf(m * m + 1.f) * -2.f + 1.f);
      }
      e[t] = v2u{pk(y[0], y[1]), pk(y[2], y[3])};
      if (j & 1) {
        if (t == 0) __builtin_amdgcn_raw_buffer_store_b64(e[0], rWS, lane * 16 + 8, aoff, SC1);
        else if ((t & 1) == 0) st16(v4u{e[t - 1].x, e[t - 1].y, e[t].x, e[t].y}, rWS, lane * 16 + (t >> 1) * 1024, aoff);
      } else {
        if (t & 1) st16(v4u{e[t - 1].x, e[t - 1].y, e[t].x, e[t].y}, rWS, lane * 16 + (t >> 1) * 1024, aoff);
        else if (t == 6) __builtin_amdgcn_raw_buffer_store_b64(e[6], rWS, lane * 16 + 3072, aoff, SC1);
      }
    }
  }
  STAMP(13);
#pragma unroll
  for (int n = W2B; n < W2B + W2A; ++n) W2L(n);
  VMWAIT((XP & 1) ? 0 : W2A);
  BAR();
  if (tid == 0) __hip_atomic_store(myflag + 768 * 32, tag, RLX_AGENT);
  STAMP(14);
#pragma unroll
  for (int n = W2B + W2A; n < 28; ++n) W2L(n);
  poll(flags + (768 + p * 16 + 2 * w + (lane & 1)) * 32, tag, 2, lane);
  STAMP(15);
  {
    const int so = WS_A + ((p * 8 + 4 * th) * 56 + 7 * w) * 1024;
    constexpr int NB = PD + 1;
    const int rt = j & 3;
    v4u f[NB];
#define ALOAD(n) __builtin_amdgcn_raw_buffer_load_b128(rWS, lane * 16 + (((n) % 7) & 3) * 1024, so + (((((n) / 7) + rt) & 3) * 56 + (((n) % 7) & 4)) * 1024, SC1)
#pragma unroll
    for (int n = 0; n < NB; ++n) { f[n] = ALOAD(n); SB(); }
    LAS char* const wr = L + (w * 1024 + 16 * g + (i ^ (4 * g))) * 16;
    f4 acc[4];
#pragma unroll
    for (int n = 0; n < 28; ++n) {
      const int s = n % 7, t = n / 7;
#pragma unroll
      for (int u = 0; u < 4; ++u) acc[u] = mfma32(w2r[s * 4 + u], f[n % NB], s ? acc[u] : z4);
      if (n + NB < 28) f[n % NB] = ALOAD(n + NB < 28 ? n + NB : 0);
      if (s == 6) {
#pragma unroll
        for (int u = 0; u < 4; ++u) *(LAS f4*)(wr + ((t + rt) & 3) * 4096 + u * 1024) = acc[u];
      }
      SB();
    }
    STAMP(16);
    BAR();
    STAMP(17);
    LAS char* const rp = opq(L + w * 2048 + (16 * pp + ((lane >> 2) ^ (4 * pp))) * 16);
    LAS char* const rq = opq(rp + 65536);
    f4 t[16];
#pragma unroll
    for (int n = 0; n < 16; ++n) t[n] = *(LAS f4*)((n & 4 ? rq : rp) + (n & 3) * 16384 + (n >> 3) * 1024);
    SB();
    const f4 a0 = ((t[0] + t[1]) + (t[2] + t[3])) + ((t[4] + t[5]) + (t[6] + t[7]));
    const f4 a1 = ((t[8] + t[9]) + (t[10] + t[11])) + ((t[12] + t[13]) + (t[14] + t[15]));
    const v4u o = {pk(a0[0] + bflo(hres.x), a0[1] + bfhi(hres.x)), pk(a0[2] + bflo(hres.y), a0[3] + bfhi(hres.y)),
                   pk(a1[0] + bflo(hres.z), a1[1] + bfhi(hres.z)), pk(a1[2] + bflo(hres.w), a1[3] + bfhi(hres.w))};
    *(v4u*)(out + (size_t)(p * 128 + th * 64 + (w >> 1) * 16 + (lane >> 2)) * 512 + 64 * c4 + 32 * hd + 8 * pp) = o;
  }
  STAMP(18);
  STAMP_FLUSH
}

extern "C" void kernel_launch(void* const* d_in, const int* in_sizes, int n_in, void* d_out, int out_size, void* d_ws, size_t ws_size,
                              hipStream_t stream) {
  (void)in_sizes; (void)n_in; (void)out_size; (void)ws_size;
  (void)hipFuncSetAttribute((const void*)fused_block, hipFuncAttributeMaxDynamicSharedMemorySize, LDS_BYTES);
  fused_block<<<256, 512, LDS_BYTES, stream>>>((const u16*)d_in[0], (const u16*)d_in[1], (const u16*)d_in[2], (const u16*)d_in[4],
                                               (const u16*)d_in[5], (const u16*)d_in[6], (u16*)d_out, (char*)d_ws);
}
```

```cpp
#include <hip/hip_runtime.h>
#include <hip/hip_bf16.h>

typedef unsigned short u16;
typedef unsigned v4u __attribute__((ext_vector_type(4)));
typedef unsigned v2u __attribute__((ext_vector_type(2)));
typedef float f4 __attribute__((ext_vector_type(4)));
typedef float f2 __attribute__((ext_vector_type(2)));
typedef short s4 __attribute__((ext_vector_type(4)));
typedef __bf16 b8 __attribute__((ext_vector_type(8)));
typedef __bf16 b2 __attribute__((ext_vector_type(2)));
#define GAS __attribute__((address_space(1)))
#define LAS __attribute__((address_space(3)))
#if defined(__HIP_DEVICE_COMPILE__)
#define TGT __attribute__((target("no-packed-fp32-ops")))
#else
#define TGT
#endif
#define DI __device__ __forceinline__ TGT
#define RLX_AGENT __ATOMIC_RELAXED, __HIP_MEMORY_SCOPE_AGENT

#define MAPMODE 1
#define XP 0
#define PD 6
#define KVAT 12
#define W2A 4

constexpr int WS_EPOCH = 0;
constexpr int WS_FLAG = 4096;
constexpr int WS_XS = 196608;
constexpr int WS_KV = 262144;
constexpr int KV_REC = 1152;
constexpr int WS_O = 1 << 20;
constexpr int WS_H = 3 << 20;
constexpr int WS_A = 5 << 20;
constexpr int WS_S = 12 << 20;
constexpr int WS_P1 = 13 << 20;
constexpr int WS_PO = 15 << 20;
constexpr int WS_PC = WS_PO + (1 << 19);
constexpr int WS_P2 = 16 << 20;
constexpr int WS_BYTES = 18 << 20;
#define STAMP(n)
#define STAMP_DECL
#define STAMP_FLUSH
#define COUNT(n)

constexpr int L_WQ = 0, L_WK = 32768, L_KVRED = 98304, L_WO = 114688, L_SCR = 147456;
constexpr int L_RAW = 0;
constexpr int L_W1 = 0;
constexpr int L_CONS = L_SCR;
constexpr int L_ZP = L_SCR + 8192;
constexpr int L_ZC = L_ZP + 512;
constexpr int L_CKV = L_ZC + 512;
constexpr int L_STT = L_CKV + 512;
constexpr int L_C1 = L_STT + 1024;
constexpr int L_B1 = L_C1 + 512;
constexpr int LDS_BYTES = 163840;

constexpr unsigned ONE2 = 0x3F803F80u;
constexpr int SC1 = 16;
constexpr float LOG2E = 1.4426950408889634f;
constexpr unsigned SPIN = 1u << 15;

DI float asf(unsigned u) { return __builtin_bit_cast(float, u); }
DI unsigned asu(float f) { return __builtin_bit_cast(unsigned, f); }
DI f4 mfma32(v4u a, v4u b, f4 c) { return __builtin_amdgcn_mfma_f32_16x16x32_bf16(__builtin_bit_cast(b8, a), __builtin_bit_cast(b8, b), c, 0, 0, 0); }
DI f4 mfma16(v2u a, v2u b, f4 c) { return __builtin_amdgcn_mfma_f32_16x16x16bf16_1k(__builtin_bit_cast(s4, a), __builtin_bit_cast(s4, b), c, 0, 0, 0); }
DI unsigned pk(float lo, float hi) { f2 v = {lo, hi}; return __builtin_bit_cast(unsigned, __builtin_convertvector(v, b2)); }
DI float bflo(unsigned u) { return asf(u << 16); }
DI float bfhi(unsigned u) { return asf(u & 0xffff0000u); }
DI v2u trd(LAS char* p) { return __builtin_bit_cast(v2u, __builtin_amdgcn_ds_read_tr16_b64_v4i16((LAS s4*)p)); }
DI v4u trfrag(LAS char* p, int second) { v2u a = trd(p), b = trd(p + second); return v4u{a.x, a.y, b.x, b.y}; }
DI v4u ldf(LAS char* p) { return *(LAS v4u*)p; }
DI float dot2(unsigned a, unsigned b, float c) { return __builtin_amdgcn_fdot2_f32_bf16(__builtin_bit_cast(b2, a), __builtin_bit_cast(b2, b), c, false); }
template <int C> DI float dppadd(float v) { return v + asf(__builtin_amdgcn_mov_dpp(asu(v), C, 0xf, 0xf, true)); }
DI float rowsum4(float v) {
  auto r = __builtin_amdgcn_permlane32_swap(asu(v), asu(v), false, false);
  const float s = asf(r[0]) + asf(r[1]);
  auto t = __builtin_amdgcn_permlane16_swap(asu(s), asu(s), false, false);
  return asf(t[0]) + asf(t[1]);
}
DI bool none(bool c) { return __builtin_amdgcn_ballot_w64(c) == 0; }
DI v4u mksrd(const void* p, unsigned bytes) {
  unsigned long long a = (unsigned long long)p;
  return v4u{(unsigned)a, (unsigned)(a >> 32) & 0xffffu, bytes, 0x00020000u};
}
DI void dma16(v4u srd, unsigned voff, unsigned soff, unsigned lds) {
  asm volatile("s_mov_b32 m0, %3\n\ts_nop 0\n\tbuffer_load_dwordx4 %0, %1, %2 offen lds" ::"v"(voff), "s"(srd), "s"(soff), "s"(lds) : "memory");
}
template <int N> DI void dmac(v4u srd, unsigned voff, unsigned so, unsigned lds) {
  if (N == 1) asm volatile("s_mov_b32 m0, %3\n\ts_nop 0\n\tbuffer_load_dwordx4 %0, %1, %2 offen sc1 lds" ::"v"(voff), "s"(srd), "s"(so), "s"(lds) : "memory");
  if (N == 3) asm volatile("s_mov_b32 m0, %3\n\ts_nop 0\n\tbuffer_load_dwordx4 %0, %1, %2 offen sc1 lds\n\tbuffer_load_dwordx4 %0, %1, %2 offen offset:1024 sc1 lds\n\t"
                           "buffer_load_dwordx4 %0, %1, %2 offen offset:2048 sc1 lds" ::"v"(voff), "s"(srd), "s"(so), "s"(lds) : "memory");
  if (N == 4) asm volatile("s_mov_b32 m0, %3\n\ts_nop 0\n\tbuffer_load_dwordx4 %0, %1, %2 offen sc1 lds\n\tbuffer_load_dwordx4 %0, %1, %2 offen offset:1024 sc1 lds\n\t"
                           "buffer_load_dwordx4 %0, %1, %2 offen offset:2048 sc1 lds\n\tbuffer_load_dwordx4 %0, %1, %2 offen offset:3072 sc1 lds"
                           ::"v"(voff), "s"(srd), "s"(so), "s"(lds) : "memory");
}
#define LD16(dst, srd, voff, soff) asm volatile("buffer_load_dwordx4 %0, %1, %2, %3 offen sc1" : "=v"(dst) : "v"(voff), "s"(srd), "s"(soff) : "memory")
DI void st16(v4u v, __amdgpu_buffer_rsrc_t r, int voff, int soff) {
  __builtin_amdgcn_raw_buffer_store_b128(v, r, voff, soff, SC1);
  asm volatile("s_nop 1" ::"v"(v) : "memory");
}
DI LAS char* opq(LAS char* p) { asm volatile("" : "+v"(p)); return p; }
#define LGKM0() asm volatile("s_waitcnt lgkmcnt(0)" ::: "memory")
#define SBAR() do { __builtin_amdgcn_s_barrier(); asm volatile("" ::: "memory"); } while (0)
#define BAR() do { LGKM0(); SBAR(); } while (0)
#define VMWAIT(n) asm volatile("s_waitcnt vmcnt(%0)" ::"i"(n) : "memory")
#define SB() __builtin_amdgcn_sched_barrier(0)

DI float phi(float t) { return fmaxf(t, 0.f) + __builtin_amdgcn_exp2f(fminf(t, 0.f) * LOG2E) + 1e-6f; }
DI void poll(GAS unsigned* fp, unsigned tag, int n, int lane) {
#pragma nounroll
  for (unsigned sp = 0; sp < SPIN; ++sp) {
    const unsigned v = lane < n ? __hip_atomic_load(fp, RLX_AGENT) : tag;
    if (none(v != tag)) break;
  }
  asm volatile("" ::: "memory");
}

__global__ void __launch_bounds__(512, 2) TGT
fused_block(const u16* __restrict__ x, const u16* __restrict__ wqkv, const u16* __restrict__ wo, const u16* __restrict__ w1,
            const u16* __restrict__ b1, const u16* __restrict__ w2, u16* __restrict__ out, char* ws) {
  extern __shared__ __attribute__((aligned(16))) char smem_[];
  LAS char* L = (LAS char*)smem_;
  const int tid = threadIdx.x, lane = tid & 63, w = __builtin_amdgcn_readfirstlane(tid >> 6);
  const int g = lane >> 4, i = lane & 15, q = (lane >> 2) & 3, pp = lane & 3;
  const int bid = blockIdx.x, xcd = bid & 7, rk = bid >> 3;
  const int p = 4 * (xcd >> 1) + (rk >> 3), j = (xcd & 1) * 8 + (rk & 7);
  const int rot = 2 * p + j;
  STAMP_DECL
  STAMP(0);
  const v4u dQKV = mksrd(wqkv, 512 * 1536 * 2), dX = mksrd(x, 2048 * 512 * 2), dWS = mksrd(ws, WS_BYTES);
  const __amdgpu_buffer_rsrc_t rWS = __builtin_amdgcn_make_buffer_rsrc(ws, 0, WS_BYTES, 0x00020000);
  const unsigned lds0 = (unsigned)(size_t)L;
  LAS char* const Ll = L + lane * 16;
  const int th = j >> 3, c4 = j & 7;

  const unsigned vW = (lane >> 2) * 3072 + (pp ^ ((lane >> 5) << 1)) * 16;
  const unsigned vX0 = (lane >> 3) * 1024 + ((lane & 7) ^ g) * 16, vX1 = (vX0 ^ 64) + 8192;
  const unsigned sX = (p * 128 + w * 16) * 1024;
  const unsigned sW = (w & 3) * (16 * 3072) + (1 + (w >> 2)) * 1024 + j * 64, lW = lds0 + L_WK + (w >> 2) * 32768 + (w & 3) * 1024;
#define XSLOT(c) ((c) % 3 == 0 ? L_KVRED + w * 2048 : L_WO + w * 4096 + ((c) % 3 - 1) * 2048)
#define WLOAD(c) dma16(dQKV, vW, sW + (((c) + rot) & 7) * (64 * 3072), lW + (c) * 4096)
#define XLOAD1(c, h) dma16(dX, (h) ? vX1 : vX0, sX + (((c) + rot) & 7) * 128, lds0 + XSLOT(c) + (h) * 1024)
#define QLOAD1(n) dma16(dQKV, vW, (((w + rot) & 7) * 64 + (n) * 16) * 3072 + j * 64, lds0 + L_WQ + w * 4096 + (n) * 1024)
  unsigned tag;
  v4u xrow;
  asm volatile("buffer_load_dwordx4 %0, %1, %2, %3 offen" : "=v"(xrow) : "v"(lane * 16), "s"(dX), "s"((p * 128 + j * 8 + w) * 1024) : "memory");
  asm volatile("buffer_load_dword %0, off, %1, 0 sc1" : "=v"(tag) : "s"(dWS) : "memory");
  WLOAD(0); XLOAD1(0, 0); XLOAD1(0, 1);
  SBAR();
  WLOAD(1); XLOAD1(1, 0); XLOAD1(1, 1);
  const int u2 = 2 * p + th + 32 * (w >> 2);
  {
    const unsigned c = w * 64 + lane, r = (c * 4682u) >> 16;
    if (w < 7) dma16(mksrd(w1, 512 * 1792 * 2), r * 3584 + (c - r * 14) * 16, p * (32 * 3584) + 224 * j, lds0 + L_RAW + w * 1024);
    if (w >= 6) dma16(mksrd(wo, 512 * 512 * 2), (lane >> 2) * 1024 + pp * 16, (p * 32 + (w & 1) * 16) * 1024 + j * 64, lds0 + L_RAW + 7168 + (w & 1) * 1024);
    dma16(mksrd(w2, 1792 * 512 * 2), (lane >> 3) * 1024 + (lane & 7) * 16, (u2 * 32 + (w & 3) * 8) * 1024 + c4 * 128, lds0 + L_RAW + 9216 + w * 1024);
  }
  STAMP(1);
  asm volatile("s_waitcnt vmcnt(8)" : "+v"(xrow), "+v"(tag) :: "memory");
  tag += 1u;
  {
    float s = dot2(xrow.x, ONE2, dot2(xrow.y, ONE2, 0.f)) + dot2(xrow.z, ONE2, dot2(xrow.w, ONE2, 0.f));
    float ss = dot2(xrow.x, xrow.x, dot2(xrow.y, xrow.y, 0.f)) + dot2(xrow.z, xrow.z, dot2(xrow.w, xrow.w, 0.f));
    s = rowsum4(dppadd<0x128>(dppadd<0x124>(dppadd<0x122>(dppadd<0x121>(s))))) * (1.f / 512.f);
    ss = rowsum4(dppadd<0x128>(dppadd<0x124>(dppadd<0x122>(dppadd<0x121>(ss))))) * (1.f / 512.f);
    const float r = __builtin_amdgcn_rsqf(fmaxf(ss - s * s, 0.f) + 1e-5f);
    if (lane == 0) st16(v4u{asu(s), tag, asu(r), tag}, rWS, 0, WS_XS + (p * 128 + j * 8 + w) * 16);
  }
  GAS unsigned* const flags = (GAS unsigned*)(ws + WS_FLAG);
  GAS unsigned* const myflag = flags + (p * 16 + j) * 32;
  const int hd = w & 1;
  LAS char* const tN = L + (8 * g + q) * 64 + pp * 8 + ((g ^ hd) & 1) * 32;
  LAS char* const kN = opq(tN + L_WK);
  LAS char* const kC = opq(tN + L_WK + ((w >> 1) & 1) * 32768 + (w >> 2) * 2048);
#define WFRAG(s, m) trfrag(kN + (m) * 32768 + (s) * 2048, 256)
  v4u ones = {ONE2, ONE2, ONE2, ONE2};
  asm volatile("" : "+v"(ones));
  v4u xf[2][16], xres;
  const f4 z4 = {0, 0, 0, 0};
  f4 ak[2] = {z4, z4}, av[2] = {z4, z4}, cs = z4;
  const int xrd = i * 128 + (g ^ (i >> 1)) * 16;
  LAS char* xp[2][2][2];
#pragma unroll
  for (int o = 0; o < 2; ++o)
#pragma unroll
    for (int h = 0; h < 2; ++h) { xp[o][0][h] = L + L_KVRED + (w ^ o) * 2048 + (xrd ^ (h * 64)); xp[o][1][h] = L + L_WO + (w ^ o) * 4096 + (xrd ^ (h * 64)); }
#define XRD(o, c, h) xf[o][2 * (c) + (h)] = ldf((c) % 3 ? xp[o][1][h] + ((c) % 3 - 1) * 2048 : xp[o][0][h])
  v4u wf[2][4], cf, xs, pr1, pro, pr2;
  f4 prc;
  f4 mr[2], rs[2];
  f2 c2k, c2v;
  const int cres = ((j >> 1) - rot) & 7;
  const int rot4 = (j & 3) * 4;
#define P1L(N, slot, n) if (!(XP & 2)) dmac<N>(dWS, lane * 16, WS_P1 + (j * 112 + (((slot) + rot4) & 15) * 7 + (n)) * 1024, lds0 + L_W1 + ((slot) * 7 + (n)) * 1024)
#pragma unroll
  for (int c = -1; c < 8; ++c) {
    if (c >= 0) {
      ak[0] = mfma32(xf[0][2 * c], wf[c & 1][0], ak[0]);
      ak[1] = mfma32(xf[1][2 * c], wf[c & 1][0], ak[1]);
      SB();
    }
    if (c < 6) VMWAIT(c < 1 ? 5 : 3);
    else if (c == 6) {
      asm volatile("s_waitcnt vmcnt(2)" : "+v"(xs) :: "memory");
#pragma nounroll
      for (unsigned sp = 0; sp < SPIN && !none(xs.y != tag || xs.w != tag); ++sp) {
        COUNT(23);
        xs = __builtin_amdgcn_raw_buffer_load_b128(rWS, i * 16, WS_XS + (p * 128 + w * 16) * 16, SC1);
        asm volatile("" ::: "memory");
      }
      if (g == 0) { *(LAS unsigned*)(L + L_STT + w * 128 + i * 4) = xs.x; *(LAS unsigned*)(L + L_STT + w * 128 + 64 + i * 4) = xs.z; }
    } else LGKM0();
    SBAR();
    if (c >= 0) {
      av[0] = mfma32(xf[0][2 * c], wf[c & 1][1], av[0]);
      av[1] = mfma32(xf[1][2 * c], wf[c & 1][1], av[1]);
    }
    if (c < 7) {
      XRD(0, c + 1, 0); XRD(1, c + 1, 0); XRD(0, c + 1, 1); XRD(1, c + 1, 1);
      cf = trfrag(kC + (c + 1) * 4096, 256);
    } else {
#pragma unroll
      for (int o = 0; o < 2; ++o) { mr[o] = *(LAS f4*)(L + L_STT + (w ^ o) * 128 + g * 16); rs[o] = *(LAS f4*)(L + L_STT + (w ^ o) * 128 + 64 + g * 16); }
      c2k = *(LAS f2*)(L + L_CKV + (hd * 16 + i) * 8); c2v = *(LAS f2*)(L + L_CKV + (32 + hd * 16 + i) * 8);
    }
    if (c < 5) WLOAD(c + 3);
    if (c == 5) LD16(xs, dWS, i * 16, WS_XS + (p * 128 + w * 16) * 16);
    SB();
    if (c >= 0) {
      ak[0] = mfma32(xf[0][2 * c + 1], wf[c & 1][2], ak[0]);
      ak[1] = mfma32(xf[1][2 * c + 1], wf[c & 1][2], ak[1]);
    }
    if (c < 7) { wf[(c + 1) & 1][0] = WFRAG(2 * c + 2, 0); wf[(c + 1) & 1][1] = WFRAG(2 * c + 2, 1); }
    if (c < 5) XLOAD1(c + 3, 0); else if (c < 7) QLOAD1(2 * c - 10);
    SB();
    if (c >= 0) {
      av[0] = mfma32(xf[0][2 * c + 1], wf[c & 1][3], av[0]);
      av[1] = mfma32(xf[1][2 * c + 1], wf[c & 1][3], av[1]);
    }
    if (c < 7) { wf[(c + 1) & 1][2] = WFRAG(2 * c + 3, 0); wf[(c + 1) & 1][3] = WFRAG(2 * c + 3, 1); }
    if (c < 5) XLOAD1(c + 3, 1); else if (c < 7) QLOAD1(2 * c - 9);
    if (c == 7) {
      st16(pr1, rWS, lane * 16, w < 7 ? WS_P1 + ((j * 16 + p) * 7 + w) * 1024 : WS_BYTES);
      st16(pr2, rWS, lane * 16, u2 < 56 ? WS_P2 + ((c4 * 56 + u2) * 4 + (w & 3)) * 1024 : WS_BYTES);
      if (i == 0) st16(__builtin_bit_cast(v4u, prc), rWS, g * 16, w < 7 ? WS_PC + ((j * 7 + w) * 16 + p) * 64 : WS_BYTES);
      if (w >= 6) st16(pro, rWS, lane * 16, WS_PO + ((j * 16 + p) * 2 + (w & 1)) * 1024);
    }
    SB();
    if (c < 7) {
      cs = mfma32(ones, cf, cs);
      if (c + 1 == cres) xres = (j & 1) ? xf[0][2 * c + 3] : xf[0][2 * c + 2];
    }
    if (c == 1) {
      const int il = (pp * 2 + (w & 1)) * 8;
      pr1 = trfrag(L + L_RAW + (8 * g + q) * 224 + pp * 8 + w * 32, 4 * 224);
      pro = trfrag(L + L_RAW + 7168 + (8 * g + q) * 64 + il, 256);
      pr2 = trfrag(L + L_RAW + 9216 + (w >> 2) * 4096 + (4 * g + q) * 128 + (w & 2) * 32 + il, 2048);
    }
    if (c == 2) prc = mfma32(pr1, ones, z4);
    if (c == 6 && g == 0) *(LAS float*)(L + L_CKV + ((w & 3) * 16 + i) * 8 + (w >> 2) * 4) = cs[0];
  }
  STAMP(3);
  {
    const float ck = c2k[0] + c2k[1], cv = c2v[0] + c2v[1];
    float kk[8], vv[8];
#pragma unroll
    for (int n = 0; n < 8; ++n) {
      kk[n] = phi((ak[n >> 2][n & 3] - mr[n >> 2][n & 3] * ck) * rs[n >> 2][n & 3]);
      vv[n] = (av[n >> 2][n & 3] - mr[n >> 2][n & 3] * cv) * rs[n >> 2][n & 3];
    }
    const v4u pkf = {pk(kk[0], kk[1]), pk(kk[2], kk[3]), pk(kk[4], kk[5]), pk(kk[6], kk[7])};
    const v4u pvf = {pk(vv[0], vv[1]), pk(vv[2], vv[3]), pk(vv[4], vv[5]), pk(vv[6], vv[7])};
    const f4 kv = mfma32(pkf, pvf, z4), z = mfma32(pkf, ones, z4);
    *(LAS f4*)(Ll + L_KVRED + w * 1024) = kv;
    if (i == 0) *(LAS f4*)(L + L_ZP + w * 64 + g * 16) = z;
  }
  if (w < 6) VMWAIT(3); else VMWAIT(4);
  BAR();
  STAMP(4);
  if (w < 2) {
    __builtin_amdgcn_s_setprio(3);
    f4 t[8];
#pragma unroll
    for (int n = 0; n < 4; ++n) { t[n] = *(LAS f4*)(Ll + L_KVRED + (n * 2 + w) * 1024); t[4 + n] = *(LAS f4*)(L + L_ZP + (n * 2 + w) * 64 + pp * 16); }
    SB();
    const int so = WS_KV + ((j * 2 + w) * 16 + p) * KV_REC;
    const f4 a = (t[0] + t[1]) + (t[2] + t[3]), z = (t[4] + t[5]) + (t[6] + t[7]);
    st16(v4u{pk(a[0], a[1]), tag, pk(a[2], a[3]), tag}, rWS, lane * 16, so);
    if (lane < 4) st16(v4u{pk(z[0], z[1]), tag, pk(z[2], z[3]), tag}, rWS, lane * 16 + 1024, so);
    __builtin_amdgcn_s_setprio(0);
    STAMP(2);
  }
  v2u pq[2];
  v4u kt[5];
  const unsigned kso = WS_KV + ((j * 2 + hd) * 16 + 4 * (w >> 1)) * KV_REC;
  {
    f4 aq[2] = {z4, z4}, cq = z4;
    v4u qf[4];
#pragma unroll
    for (int s = 0; s < 3; ++s) qf[s] = trfrag(tN + L_WQ + s * 2048, 256);
#pragma unroll
    for (int s = 0; s < 16; ++s) {
      if (s + 3 < 16) qf[(s + 3) & 3] = trfrag(tN + L_WQ + (s + 3) * 2048, 256);
      aq[0] = mfma32(qf[s & 3], xf[0][s], aq[0]);
      aq[1] = mfma32(qf[s & 3], xf[1][s], aq[1]);
      cq = mfma32(qf[s & 3], ones, cq);
      if (s == 8) {
        if (w < 2) VMWAIT(2); else VMWAIT(0);
        SBAR();
        if (tid == 128) __hip_atomic_store(myflag, tag, RLX_AGENT);
      }
      if (s == KVAT) {
#pragma unroll
        for (int n = 0; n < 4; ++n) LD16(kt[n], dWS, lane * 16, kso + n * KV_REC);
        LD16(kt[4], dWS, q * KV_REC + pp * 16 + 1024, kso);
      }
      SB();
    }
#pragma unroll
    for (int o = 0; o < 2; ++o) {
      const float m = *(LAS float*)(L + L_STT + (w ^ o) * 128 + i * 4), r = *(LAS float*)(L + L_STT + (w ^ o) * 128 + 64 + i * 4);
      float a[4];
#pragma unroll
      for (int n = 0; n < 4; ++n) a[n] = phi((aq[o][n] - m * cq[n]) * r);
      pq[o] = v2u{pk(a[0], a[1]), pk(a[2], a[3])};
    }
    asm volatile("" : "+v"(pq[0]), "+v"(pq[1]));
  }
  STAMP(5);
  {
    asm volatile("s_waitcnt vmcnt(0)" : "+v"(kt[0]), "+v"(kt[1]), "+v"(kt[2]), "+v"(kt[3]), "+v"(kt[4]) :: "memory");
#pragma nounroll
    for (unsigned sp = 0; sp < SPIN; ++sp) {
      unsigned bad = 0;
#pragma unroll
      for (int n = 0; n < 5; ++n) bad |= (kt[n].y ^ tag) | (kt[n].w ^ tag);
      if (none(bad != 0)) break;
      COUNT(24);
#pragma unroll
      for (int n = 0; n < 4; ++n) kt[n] = __builtin_amdgcn_raw_buffer_load_b128(rWS, lane * 16, kso + n * KV_REC, SC1);
      kt[4] = __builtin_amdgcn_raw_buffer_load_b128(rWS, q * KV_REC + pp * 16 + 1024, kso, SC1);
      asm volatile("" ::: "memory");
    }
    STAMP(6);
    const f4 s = {(bflo(kt[0].x) + bflo(kt[1].x)) + (bflo(kt[2].x) + bflo(kt[3].x)), (bfhi(kt[0].x) + bfhi(kt[1].x)) + (bfhi(kt[2].x) + bfhi(kt[3].x)),
                  (bflo(kt[0].z) + bflo(kt[1].z)) + (bflo(kt[2].z) + bflo(kt[3].z)), (bfhi(kt[0].z) + bfhi(kt[1].z)) + (bfhi(kt[2].z) + bfhi(kt[3].z))};
    *(LAS f4*)(Ll + L_CONS + w * 1024) = s;
    const f4 z = {dppadd<0x128>(dppadd<0x124>(bflo(kt[4].x))), dppadd<0x128>(dppadd<0x124>(bfhi(kt[4].x))),
                  dppadd<0x128>(dppadd<0x124>(bflo(kt[4].z))), dppadd<0x128>(dppadd<0x124>(bfhi(kt[4].z)))};
    if (lane < 4) *(LAS f4*)(L + L_ZC + w * 64 + lane * 16) = z;
  }
  BAR();
  STAMP(7);
  unsigned pf = tag;
  if (w == 0) asm volatile("buffer_load_dword %0, %1, %2, %3 offen sc1" : "+v"(pf) : "v"((i * 16 + (j ^ ((g & 1) << 3))) * 128), "s"(dWS), "s"(WS_FLAG) : "memory");
  const int ohoff = ((p * 8 + w) * 16 + j) * 1024;
  {
    f4 kq[4], zq[4];
#pragma unroll
    for (int n = 0; n < 4; ++n) { kq[n] = *(LAS f4*)(Ll + L_CONS + (2 * n + hd) * 1024); zq[n] = *(LAS f4*)(L + L_ZC + (2 * n + hd) * 64 + g * 16); }
    SB();
    const f4 k0 = (kq[0] + kq[1]) + (kq[2] + kq[3]), z0 = (zq[0] + zq[1]) + (zq[2] + zq[3]);
    const v2u ka = {pk(k0[0], k0[1]), pk(k0[2], k0[3])}, za = {pk(z0[0], z0[1]), pk(z0[2], z0[3])};
    const f4 n0 = mfma16(ka, pq[0], z4), n1 = mfma16(ka, pq[1], z4), d0 = mfma16(za, pq[0], z4), d1 = mfma16(za, pq[1], z4);
    const float r0 = __builtin_amdgcn_rcpf(d0[0] + 1e-6f), r1 = __builtin_amdgcn_rcpf(d1[0] + 1e-6f);
    const auto sx = __builtin_amdgcn_permlane16_swap(pk(n0[0] * r0, n0[1] * r0), pk(n1[0] * r1, n1[1] * r1), false, false);
    const auto sy = __builtin_amdgcn_permlane16_swap(pk(n0[2] * r0, n0[3] * r0), pk(n1[2] * r1, n1[3] * r1), false, false);
    st16(v4u{sx[0], sy[0], sx[1], sy[1]}, rWS, ((hd ^ g) & 1) * 16384 + ((2 * hd + (g >> 1)) * 16 + i) * 16, WS_O + ohoff - hd * 16384);
  }
  asm volatile("s_waitcnt vmcnt(0)" : "+v"(pf) :: "memory");
  if (!none(pf != tag)) poll(flags + (i * 16 + (j ^ ((g & 1) << 3))) * 32, tag, 32, lane);
  SBAR();
  if (tid == 0) __hip_atomic_store(myflag + 256 * 32, tag, RLX_AGENT);
  STAMP(20);
  {
    const v4u ct = __builtin_amdgcn_raw_buffer_load_b128(rWS, lane * 16, WS_PC + (j * 7 + (w ? w - 1 : 0)) * 1024, SC1);
    const unsigned bias = __builtin_amdgcn_raw_buffer_load_b16(__builtin_amdgcn_make_buffer_rsrc((void*)(b1 + 112 * j), 0, 224, 0x00020000), (tid - 64) * 2, 0, 0);
    if (!(XP & 2)) dmac<4>(dWS, lane * 16, WS_PO + (j * 32 + ((2 * w + rot4) & 15) * 2) * 1024, lds0 + L_WO + w * 4096);
    P1L(4, 2 * w, 0);
    if (w) {
      const f4 c = {rowsum4(dppadd<0x128>(dppadd<0x124>(asf(ct.x)))), rowsum4(dppadd<0x128>(dppadd<0x124>(asf(ct.y)))),
                    rowsum4(dppadd<0x128>(dppadd<0x124>(asf(ct.z)))), rowsum4(dppadd<0x128>(dppadd<0x124>(asf(ct.w))))};
      if (lane < 4) *(LAS f4*)(L + L_C1 + (w - 1) * 64 + lane * 16) = c;
      if (w < 3) *(LAS float*)(L + L_B1 + (tid - 64) * 4) = bflo(bias);
    } else poll(flags + (256 + p * 16 + i) * 32, tag, 16, lane);
  }
  VMWAIT(0);
  SBAR();
  STAMP(8);
#define LOAD1(s) f[s] = __builtin_amdgcn_raw_buffer_load_b128(rWS, lane * 16 + ((s) & 3) * 1024, so + ((((s) & 12) + rot4) & 15) * 1024, SC1)
#define LOADPD() do { _Pragma("unroll") for (int s = 0; s < PD; ++s) { LOAD1(s); SB(); } } while (0)
  {
    const int so = WS_O + (p * 8 + w) * 16384;
    v4u f[16];
    LAS char* const Lo = opq(Ll + L_WO);
    LOADPD();
    f4 a0 = {bflo(xres.x), bfhi(xres.x), bflo(xres.y), bfhi(xres.y)}, a1 = {bflo(xres.z), bfhi(xres.z), bflo(xres.w), bfhi(xres.w)};
    v4u wf2[3][2];
#pragma unroll
    for (int s = 0; s < 2; ++s) { wf2[s][0] = ldf(Lo + s * 2048); wf2[s][1] = ldf(Lo + s * 2048 + 1024); }
#pragma unroll
    for (int s = 0; s < 16; ++s) {
      if (s + PD < 16) LOAD1(s + PD < 16 ? s + PD : 0);
      if (s + 2 < 16) { wf2[(s + 2) % 3][0] = ldf(Lo + (s + 2) * 2048); wf2[(s + 2) % 3][1] = ldf(Lo + (s + 2) * 2048 + 1024); }
      SB();
      a0 = mfma32(wf2[s % 3][0], f[s], a0);
      a1 = mfma32(wf2[s % 3][1], f[s], a1);
      SB();
    }
    P1L(3, 2 * w, 4);
    const v4u h = {pk(a0[0], a0[1]), pk(a0[2], a0[3]), pk(a1[0], a1[1]), pk(a1[2], a1[3])};
    st16(h, rWS, lane * 16, WS_H + ohoff);
    const f4 hs = mfma32(ones, h, z4), hg = mfma32(h, h, z4);
    if (g == q) __builtin_amdgcn_raw_buffer_store_b64(v2u{asu(hs[0]), asu(pp == 0 ? hg[0] : pp == 1 ? hg[1] : pp == 2 ? hg[2] : hg[3])}, rWS, i * 8, WS_S + (ohoff >> 3), SC1);
    if (bid == 0 && tid == 0) __builtin_amdgcn_raw_buffer_store_b32(tag, rWS, WS_EPOCH, 0, SC1);
  }
  STAMP(9);
  VMWAIT(0);
  BAR();
  if (tid == 0) __hip_atomic_store(myflag + 512 * 32, tag, RLX_AGENT);
  STAMP(21);
  v4u w2r[28];
  const int sW2 = WS_P2 + (c4 * 56 + 7 * w) * 4096;
#define W2L(n) w2r[n] = __builtin_amdgcn_raw_buffer_load_b128(rWS, lane * 16 + ((n) & 3) * 1024, sW2 + ((n) >> 2) * 4096, SC1)
  P1L(4, 2 * w + 1, 0); P1L(3, 2 * w + 1, 4);
  if (w == 0) poll(flags + (512 + p * 16 + i) * 32, tag, 16, lane);
  VMWAIT(0);
  SBAR();
  STAMP(10);
  v4u hres;
  constexpr int W2B = 28 - (21 - PD);
  {
    v4u f[16];
    const int so = WS_H + (p * 8 + w) * 16384;
    LAS char* const wp0 = opq(Ll + L_W1);
    LAS char* const wp1 = opq(wp0 + 8 * 7168);
#define W1F(n) ldf(((n) < 56 ? wp0 : wp1) + ((n) % 56) * 1024)
    f4 acc[7];
    LOADPD();
    v2u st[4];
#pragma unroll
    for (int t = 0; t < 7; ++t) acc[t] = z4;
    {
      v4u wf3[6];
#pragma unroll
      for (int n = 0; n < 4; ++n) wf3[n] = W1F(n);
#pragma unroll
      for (int n = 0; n < 112; n += 2) {
        if (n + 4 < 112) { wf3[(n + 4) % 6] = W1F(n + 4); wf3[(n + 5) % 6] = W1F(n + 5); }
        SB();
        acc[(n + 1) % 7] = mfma32(wf3[(n + 1) % 6], f[(n + 1) / 7], acc[(n + 1) % 7]);
        acc[n % 7] = mfma32(wf3[n % 6], f[n / 7], acc[n % 7]);
        const int k = n / 4 + PD;
        if (k < 21) {
          if ((n & 2) == 0) {
            if (k < 16) LOAD1(k < 16 ? k : 0);
            else if (k < 20) st[k & 3] = __builtin_amdgcn_raw_buffer_load_b64(rWS, (4 * g + (k & 3)) * 128 + i * 8, WS_S + (p * 8 + w) * 2048, SC1);
            else hres = __builtin_amdgcn_raw_buffer_load_b128(rWS, (pp * 16 + (lane >> 2)) * 16, WS_H + ((p * 8 + 4 * th + (w >> 1)) * 16 + 2 * c4 + hd) * 1024, SC1);
          }
        } else if ((n & 2) == 0) W2L(k - 21);
        SB();
      }
    }
    f4 cq[7], bq[7];
#pragma unroll
    for (int t = 0; t < 7; ++t) { cq[t] = *(LAS f4*)(L + L_C1 + (t * 16 + 4 * g) * 4); bq[t] = *(LAS f4*)(L + L_B1 + (t * 16 + 4 * g) * 4); }
    const float mean2 = rowsum4((asf(st[0].x) + asf(st[1].x)) + (asf(st[2].x) + asf(st[3].x))) * (1.f / 512.f);
    const float var = fmaxf(rowsum4((asf(st[0].y) + asf(st[1].y)) + (asf(st[2].y) + asf(st[3].y))) * (1.f / 512.f) - mean2 * mean2, 0.f) + 1e-5f;
    const float rstd2 = __builtin_amdgcn_rsqf(var), nm = -mean2 * rstd2;
    STAMP(11);
    v2u e[7];
    const int aoff = WS_A + ((p * 8 + w) * 56 + ((7 * j) >> 1)) * 1024;
#pragma unroll
    for (int t = 0; t < 7; ++t) {
      float y[4];
#pragma unroll
      for (int r = 0; r < 4; ++r) {
        const float v = acc[t][r] * rstd2 + (nm * cq[t][r] + bq[t][r]);
        const float m = __builtin_amdgcn_exp2f(v * LOG2E) + 1.f;
        y[r] = v * (__builtin_amdgcn_rcpf(m * m + 1.f) * -2.f + 1.f);
      }
      e[t] = v2u{pk(y[0], y[1]), pk(y[2], y[3])};
      if (j & 1) {
        if (t == 0) __builtin_amdgcn_raw_buffer_store_b64(e[0], rWS, lane * 16 + 8, aoff, SC1);
        else if ((t & 1) == 0) st16(v4u{e[t - 1].x, e[t - 1].y, e[t].x, e[t].y}, rWS, lane * 16 + (t >> 1) * 1024, aoff);
      } else {
        if (t & 1) st16(v4u{e[t - 1].x, e[t - 1].y, e[t].x, e[t].y}, rWS, lane * 16 + (t >> 1) * 1024, aoff);
        else if (t == 6) __builtin_amdgcn_raw_buffer_store_b64(e[6], rWS, lane * 16 + 3072, aoff, SC1);
      }
    }
  }
  STAMP(13);
#pragma unroll
  for (int n = W2B; n < W2B + W2A; ++n) W2L(n);
  VMWAIT((XP & 1) ? 0 : W2A);
  BAR();
  if (tid == 0) __hip_atomic_store(myflag + 768 * 32, tag, RLX_AGENT);
  STAMP(14);
#pragma unroll
  for (int n = W2B + W2A; n < 28; ++n) W2L(n);
  poll(flags + (768 + p * 16 + 2 * w + (lane & 1)) * 32, tag, 2, lane);
  STAMP(15);
  {
    const int so = WS_A + ((p * 8 + 4 * th) * 56 + 7 * w) * 1024;
    constexpr int NB = PD + 1;
    const int rt = j & 3;
    v4u f[NB];
#define ALOAD(n) __builtin_amdgcn_raw_buffer_load_b128(rWS, lane * 16 + (((n) % 7) & 3) * 1024, so + (((((n) / 7) + rt) & 3) * 56 + (((n) % 7) & 4)) * 1024, SC1)
#pragma unroll
    for (int n = 0; n < NB; ++n) { f[n] = ALOAD(n); SB(); }
    LAS char* const wr = L + (w * 1024 + 16 * g + (i ^ (4 * g))) * 16;
    f4 acc[4];
#pragma unroll
    for (int n = 0; n < 28; ++n) {
      const int s = n % 7, t = n / 7;
#pragma unroll
      for (int u = 0; u < 4; ++u) acc[u] = mfma32(w2r[s * 4 + u], f[n % NB], s ? acc[u] : z4);
      if (n + NB < 28) f[n % NB] = ALOAD(n + NB < 28 ? n + NB : 0);
      if (s == 6) {
#pragma unroll
        for (int u = 0; u < 4; ++u) *(LAS f4*)(wr + ((t + rt) & 3) * 4096 + u * 1024) = acc[u];
      }
      SB();
    }
    STAMP(16);
    BAR();
    STAMP(17);
    LAS char* const rp = opq(L + w * 2048 + (16 * pp + ((lane >> 2) ^ (4 * pp))) * 16);
    LAS char* const rq = opq(rp + 65536);
    f4 t[16];
#pragma unroll
    for (int n = 0; n < 16; ++n) t[n] = *(LAS f4*)((n & 4 ? rq : rp) + (n & 3) * 16384 + (n >> 3) * 1024);
    SB();
    const f4 a0 = ((t[0] + t[1]) + (t[2] + t[3])) + ((t[4] + t[5]) + (t[6] + t[7]));
    const f4 a1 = ((t[8] + t[9]) + (t[10] + t[11])) + ((t[12] + t[13]) + (t[14] + t[15]));
    const v4u o = {pk(a0[0] + bflo(hres.x), a0[1] + bfhi(hres.x)), pk(a0[2] + bflo(hres.y), a0[3] + bfhi(hres.y)),
                   pk(a1[0] + bflo(hres.z), a1[1] + bfhi(hres.z)), pk(a1[2] + bflo(hres.w), a1[3] + bfhi(hres.w))};
    __builtin_amdgcn_raw_buffer_store_b128(o, __builtin_amdgcn_make_buffer_rsrc(out, 0, 2048 * 512 * 2, 0x00020000), ((p * 128 + th * 64 + (w >> 1) * 16 + (lane >> 2)) * 512 + 64 * c4 + 32 * hd + 8 * pp) * 2, 0, 17);
  }
  STAMP(18);
  STAMP_FLUSH
}

extern "C" void kernel_launch(void* const* d_in, const int* in_sizes, int n_in, void* d_out, int out_size, void* d_ws, size_t ws_size,
                              hipStream_t stream) {
  (void)in_sizes; (void)n_in; (void)out_size; (void)ws_size;
  (void)hipFuncSetAttribute((const void*)fused_block, hipFuncAttributeMaxDynamicSharedMemorySize, LDS_BYTES);
  fused_block<<<256, 512, LDS_BYTES, stream>>>((const u16*)d_in[0], (const u16*)d_in[1], (const u16*)d_in[2], (const u16*)d_in[4],
                                               (const u16*)d_in[5], (const u16*)d_in[6], (u16*)d_out, (char*)d_ws);
}
```

```cpp
#include <hip/hip_runtime.h>
#include <hip/hip_bf16.h>

typedef unsigned short u16;
typedef unsigned v4u __attribute__((ext_vector_type(4)));
typedef unsigned v2u __attribute__((ext_vector_type(2)));
typedef float f4 __attribute__((ext_vector_type(4)));
typedef float f2 __attribute__((ext_vector_type(2)));
typedef short s4 __attribute__((ext_vector_type(4)));
typedef __bf16 b8 __attribute__((ext_vector_type(8)));
typedef __bf16 b2 __attribute__((ext_vector_type(2)));
#define GAS __attribute__((address_space(1)))
#define LAS __attribute__((address_space(3)))
#define NOPK 1
#if defined(__HIP_DEVICE_COMPILE__) && NOPK
#define TGT __attribute__((target("no-packed-fp32-ops")))
#else
#define TGT
#endif
#define DI __device__ __forceinline__ TGT
#define RLX_AGENT __ATOMIC_RELAXED, __HIP_MEMORY_SCOPE_AGENT

#define MAPMODE 1
#define XP 0
#define OUTAUX 17
#define RAWPOL "nt"
#define RAWAT -1
#define CODE_KB 22
#define NOB 0
#define NB4 7
#define PRIO4 0
#define W2Q 4
#define W2R 4
#define W2S 4
#define PRIO3 1
#define W2P 12
#define W2X 4
#define PD 6
#define KVAT 12
#define W2A 4

constexpr int WS_EPOCH = 0;
constexpr int WS_FLAG = 4096;
constexpr int WS_XS = 196608;
constexpr int WS_KV = 262144;
constexpr int KV_REC = 1152;
constexpr int WS_O = 1 << 20;
constexpr int WS_H = 3 << 20;
constexpr int WS_A = 5 << 20;
constexpr int WS_S = 12 << 20;
constexpr int WS_P1 = 13 << 20;
constexpr int WS_PO = 15 << 20;
constexpr int WS_PC = WS_PO + (1 << 19);
constexpr int WS_BYTES = 16 << 20;
#define STAMP(n)
#define STAMP_DECL
#define STAMP_FLUSH
#define COUNT(n)

constexpr int L_WQ = 0, L_WK = 32768, L_KVRED = 98304, L_WO = 114688, L_SCR = 147456;
constexpr int L_RAW = 0;
constexpr int L_W1 = 0;
constexpr int L_CONS = L_SCR;
constexpr int L_ZP = L_SCR + 8192;
constexpr int L_ZC = L_ZP + 512;
constexpr int L_CKV = L_ZC + 512;
constexpr int L_STT = L_CKV + 512;
constexpr int L_C1 = L_STT + 1024;
constexpr int L_CQ = L_C1 + 2048;
constexpr int L_HRES = 131072;
constexpr int LDS_BYTES = 163840;

constexpr unsigned ONE2 = 0x3F803F80u;
constexpr int SC1 = 16;
constexpr float LOG2E = 1.4426950408889634f;
constexpr unsigned SPIN = 1u << 15;

DI float asf(unsigned u) { return __builtin_bit_cast(float, u); }
DI unsigned asu(float f) { return __builtin_bit_cast(unsigned, f); }
DI f4 mfma32(v4u a, v4u b, f4 c) { return __builtin_amdgcn_mfma_f32_16x16x32_bf16(__builtin_bit_cast(b8, a), __builtin_bit_cast(b8, b), c, 0, 0, 0); }
DI f4 mfma16(v2u a, v2u b, f4 c) { return __builtin_amdgcn_mfma_f32_16x16x16bf16_1k(__builtin_bit_cast(s4, a), __builtin_bit_cast(s4, b), c, 0, 0, 0); }
DI unsigned pk(float lo, float hi) { f2 v = {lo, hi}; return __builtin_bit_cast(unsigned, __builtin_convertvector(v, b2)); }
DI float bflo(unsigned u) { return asf(u << 16); }
DI float bfhi(unsigned u) { return asf(u & 0xffff0000u); }
DI v2u trd(LAS char* p) { return __builtin_bit_cast(v2u, __builtin_amdgcn_ds_read_tr16_b64_v4i16((LAS s4*)p)); }
DI v4u trfrag(LAS char* p, int second) { v2u a = trd(p), b = trd(p + second); return v4u{a.x, a.y, b.x, b.y}; }
DI v4u ldf(LAS char* p) { return *(LAS v4u*)p; }
DI float dot2(unsigned a, unsigned b, float c) { return __builtin_amdgcn_fdot2_f32_bf16(__builtin_bit_cast(b2, a), __builtin_bit_cast(b2, b), c, false); }
template <int C> DI float dppadd(float v) { return v + asf(__builtin_amdgcn_mov_dpp(asu(v), C, 0xf, 0xf, true)); }
DI float rowsum4(float v) {
  auto r = __builtin_amdgcn_permlane32_swap(asu(v), asu(v), false, false);
  const float s = asf(r[0]) + asf(r[1]);
  auto t = __builtin_amdgcn_permlane16_swap(asu(s), asu(s), false, false);
  return asf(t[0]) + asf(t[1]);
}
DI bool none(bool c) { return __builtin_amdgcn_ballot_w64(c) == 0; }
DI v4u mksrd(const void* p, unsigned bytes) {
  unsigned long long a = (unsigned long long)p;
  return v4u{(unsigned)a, (unsigned)(a >> 32) & 0xffffu, bytes, 0x00020000u};
}
DI void dma16(v4u srd, unsigned voff, unsigned soff, unsigned lds) {
  asm volatile("s_mov_b32 m0, %3\n\ts_nop 0\n\tbuffer_load_dwordx4 %0, %1, %2 offen lds" ::"v"(voff), "s"(srd), "s"(soff), "s"(lds) : "memory");
}
DI void dma16nt(v4u srd, unsigned voff, unsigned soff, unsigned lds) {
  asm volatile("s_mov_b32 m0, %3\n\ts_nop 0\n\tbuffer_load_dwordx4 %0, %1, %2 offen " RAWPOL " lds" ::"v"(voff), "s"(srd), "s"(soff), "s"(lds) : "memory");
}
template <int N> DI void dmac(v4u srd, unsigned voff, unsigned so, unsigned lds) {
  if (N == 1) asm volatile("s_mov_b32 m0, %3\n\ts_nop 0\n\tbuffer_load_dwordx4 %0, %1, %2 offen sc1 lds" ::"v"(voff), "s"(srd), "s"(so), "s"(lds) : "memory");
  if (N == 3) asm volatile("s_mov_b32 m0, %3\n\ts_nop 0\n\tbuffer_load_dwordx4 %0, %1, %2 offen sc1 lds\n\tbuffer_load_dwordx4 %0, %1, %2 offen offset:1024 sc1 lds\n\t"
                           "buffer_load_dwordx4 %0, %1, %2 offen offset:2048 sc1 lds" ::"v"(voff), "s"(srd), "s"(so), "s"(lds) : "memory");
  if (N == 4) asm volatile("s_mov_b32 m0, %3\n\ts_nop 0\n\tbuffer_load_dwordx4 %0, %1, %2 offen sc1 lds\n\tbuffer_load_dwordx4 %0, %1, %2 offen offset:1024 sc1 lds\n\t"
                           "buffer_load_dwordx4 %0, %1, %2 offen offset:2048 sc1 lds\n\tbuffer_load_dwordx4 %0, %1, %2 offen offset:3072 sc1 lds"
                           ::"v"(voff), "s"(srd), "s"(so), "s"(lds) : "memory");
}
#define LD16(dst, srd, voff, soff) asm volatile("buffer_load_dwordx4 %0, %1, %2, %3 offen sc1" : "=v"(dst) : "v"(voff), "s"(srd), "s"(soff) : "memory")
DI void st16(v4u v, __amdgpu_buffer_rsrc_t r, int voff, int soff) {
  __builtin_amdgcn_raw_buffer_store_b128(v, r, voff, soff, SC1);
  asm volatile("s_nop 1" ::"v"(v) : "memory");
}
DI LAS char* opq(LAS char* p) { asm volatile("" : "+v"(p)); return p; }
#define LGKM0() asm volatile("s_waitcnt lgkmcnt(0)" ::: "memory")
#define SBAR() do { __builtin_amdgcn_s_barrier(); asm volatile("" ::: "memory"); } while (0)
#define BAR() do { LGKM0(); SBAR(); } while (0)
#define VMWAIT(n) asm volatile("s_waitcnt vmcnt(%0)" ::"i"(n) : "memory")
#define SB() __builtin_amdgcn_sched_barrier(0)

template <int V> struct IC { static constexpr int value = V; };
template <int B, int E, int S, class F> DI void sfor(F&& f) { if constexpr (B < E) { f(IC<B>{}); sfor<B + S, E, S>(f); } }
#define SFOR(n, B, E, S) sfor<B, E, S>([&](auto n##_) __attribute__((always_inline)) TGT { constexpr int n = decltype(n##_)::value;
#define SEND });
DI float phi(float t) { return fmaxf(t, 0.f) + __builtin_amdgcn_exp2f(fminf(t, 0.f) * LOG2E) + 1e-6f; }
DI void poll(GAS unsigned* fp, unsigned tag, int n, int lane) {
#pragma nounroll
  for (unsigned sp = 0; sp < SPIN; ++sp) {
    const unsigned v = lane < n ? __hip_atomic_load(fp, RLX_AGENT) : tag;
    if (none(v != tag)) break;
  }
  asm volatile("" ::: "memory");
}

DI void w2pack(v4u* r) {
  v2u lo[4], hi[4];
#pragma unroll
  for (int n = 0; n < 4; ++n)
#pragma unroll
    for (int d = 0; d < 2; ++d) {
      const unsigned a = r[n][d], b = r[n][2 + d];
      lo[n][d] = __builtin_amdgcn_update_dpp(a, b, 0x128, 0xf, 0xc, false);
      hi[n][d] = __builtin_amdgcn_update_dpp(b, a, 0x128, 0xf, 0x3, false);
    }
#pragma unroll
  for (int u = 0; u < 4; ++u) {
    const unsigned sel = (u & 1) ? 0x07060302u : 0x05040100u;
    r[u] = v4u{__builtin_amdgcn_perm(lo[1][u >> 1], lo[0][u >> 1], sel), __builtin_amdgcn_perm(lo[3][u >> 1], lo[2][u >> 1], sel),
               __builtin_amdgcn_perm(hi[1][u >> 1], hi[0][u >> 1], sel), __builtin_amdgcn_perm(hi[3][u >> 1], hi[2][u >> 1], sel)};
  }
  asm volatile("" : "+v"(r[0]), "+v"(r[1]), "+v"(r[2]), "+v"(r[3]));
}

__global__ void __launch_bounds__(512, 2) TGT
fused_block(const u16* x_, const u16* wqkv_, const u16* wo_, const u16* w1_, const u16* b1_, const u16* w2_, u16* out_, char* ws_) {
  extern __shared__ __attribute__((aligned(16))) char smem_[];
  typedef unsigned long long u64x8 __attribute__((ext_vector_type(8)));
  u64x8 ka;
  asm volatile("s_load_dwordx16 %0, %1, 0x0" : "=s"(ka) : "s"(__builtin_amdgcn_kernarg_segment_ptr()) : "memory");
  unsigned tch = 0;
  if ((blockIdx.x >> 3) < CODE_KB && threadIdx.x < 8) {
    unsigned long long pc;
    asm volatile("s_getpc_b64 %0" : "=s"(pc));
    asm volatile("global_load_dword %0, %1, %2" : "+v"(tch) : "v"(threadIdx.x * 128), "s"((pc & ~127ull) + (blockIdx.x >> 3) * 1024) : "memory");
  }
  LAS char* L = (LAS char*)smem_;
  const int tid = threadIdx.x, lane = tid & 63, w = __builtin_amdgcn_readfirstlane(tid >> 6);
  const int g = lane >> 4, i = lane & 15, q = (lane >> 2) & 3, pp = lane & 3;
  const int bid = blockIdx.x, xcd = bid & 7, rk = bid >> 3;
  const int p = 4 * (xcd >> 1) + (rk >> 3), j = (xcd & 1) * 8 + (rk & 7);
  const int rot = 2 * p + j;
  STAMP_DECL
  STAMP(0);
  const unsigned lds0 = (unsigned)(size_t)L;
  LAS char* const Ll = L + lane * 16;
  const int th = j >> 3, c4 = j & 7;
  const int pos1 = w < (j & 1) ? 6 : w - (j & 1);

  unsigned vW = (lane >> 2) * 3072 + (pp ^ ((lane >> 5) << 1)) * 16;
  unsigned vX0 = (lane >> 3) * 1024 + ((lane & 7) ^ g) * 16, vX1 = (vX0 ^ 64) + 8192;
  unsigned sX = (p * 128 + w * 16) * 1024;
  unsigned sW = (w & 3) * (16 * 3072) + (1 + (w >> 2)) * 1024 + j * 64, lW = lds0 + L_WK + (w >> 2) * 32768 + (w & 3) * 1024;
#define XSLOT(c) ((c) % 3 == 0 ? L_KVRED + w * 2048 : L_WO + w * 4096 + ((c) % 3 - 1) * 2048)
#define WLOAD(c) dma16(dQKV, vW, sW + (((c) + rot) & 7) * (64 * 3072), lW + (c) * 4096)
#define XLOAD1(c, h) dma16(dX, (h) ? vX1 : vX0, sX + (((c) + rot) & 7) * 128, lds0 + XSLOT(c) + (h) * 1024)
#define QLOAD1(n) dma16(dQKV, vW, (((w + rot) & 7) * 64 + (n) * 16) * 3072 + j * 64, lds0 + L_WQ + w * 4096 + (n) * 1024)
  unsigned to = WS_EPOCH;
  asm("v_writelane_b32 %0, %1, 1\n\tv_writelane_b32 %0, %2, 2\n\tv_writelane_b32 %0, %3, 3\n\tv_writelane_b32 %0, %4, 4" : "+v"(to)
      : "s"(WS_XS + (p * 16 + j) * 128), "s"(WS_PC + ((j * 7 + pos1) * 16 + p) * 64), "s"(WS_S + ((p * 8 + w) * 16 + j) * 128), "s"(WS_FLAG + ((w & 3) * 256 + p * 16 + j) * 128));
  asm volatile("s_waitcnt lgkmcnt(0)" : "+s"(ka), "+v"(vW), "+v"(vX0), "+v"(vX1), "+s"(sX), "+s"(sW), "+s"(lW), "+v"(to) :: "memory");
  const u16* const x = (const u16*)ka[0]; const u16* const wqkv = (const u16*)ka[1]; const u16* const wo = (const u16*)ka[2]; const u16* const w1 = (const u16*)ka[3];
  const u16* const b1 = (const u16*)ka[4]; const u16* const w2 = (const u16*)ka[5]; u16* const out = (u16*)ka[6]; char* const ws = (char*)ka[7];
  const v4u dQKV = mksrd(wqkv, 512 * 1536 * 2), dX = mksrd(x, 2048 * 512 * 2), dWS = mksrd(ws, WS_BYTES);
  const __amdgpu_buffer_rsrc_t rWS = __builtin_amdgcn_make_buffer_rsrc(ws, 0, WS_BYTES, 0x00020000);
  WLOAD(0); XLOAD1(0, 0); XLOAD1(0, 1);
  unsigned tag;
  v4u xrow;
  asm volatile("buffer_load_dwordx4 %0, %1, %2, %3 offen" : "=v"(xrow) : "v"(lane * 16), "s"(dX), "s"((p * 128 + j * 8 + w) * 1024) : "memory");
  asm volatile("buffer_load_dword %0, %1, %2, 0 offen sc1" : "=v"(tag) : "v"(to), "s"(dWS) : "memory");
  if (w == 7) asm volatile("buffer_load_dword %0, %1, %2, %3 offen" : "+v"(tch) : "v"(((lane >> 3) & 1) * 3072 + (lane & 7) * 128), "s"(dQKV), "s"(bid * 6144) : "memory");
  SBAR();
  WLOAD(1); XLOAD1(1, 0); XLOAD1(1, 1);
  auto rawload = [&]() TGT {
    const unsigned c = w * 64 + lane, r = (c * 4682u) >> 16;
    if (w < 7) dma16nt(mksrd(w1, 512 * 1792 * 2), r * 3584 + (c - r * 14) * 16, p * (32 * 3584) + 224 * j, lds0 + L_RAW + w * 1024);
    if (w >= 6) dma16nt(mksrd(wo, 512 * 512 * 2), (lane >> 2) * 1024 + pp * 16, (p * 32 + (w & 1) * 16) * 1024 + j * 64, lds0 + L_RAW + 7168 + (w & 1) * 1024);
    asm volatile("global_load_dword %0, %1, %2" : "+v"(tch) : "v"((lane < 7 ? lane : 0) * 128), "s"((const char*)w2 + (bid * 8 + w) * 896) : "memory");
  };
  STAMP(1);
  GAS unsigned* const flags = (GAS unsigned*)(ws + WS_FLAG);
  GAS unsigned* const myflag = flags + (p * 16 + j) * 32;
  const int hd = w & 1;
  LAS char* const tN = L + (8 * g + q) * 64 + pp * 8 + ((g ^ hd) & 1) * 32;
  LAS char* const kN = opq(tN + L_WK);
  LAS char* const kC = opq(tN + L_WK + ((w >> 1) & 1) * 32768 + (w >> 2) * 2048);
#define WFRAG(s, m) trfrag(kN + (m) * 32768 + (s) * 2048, 256)
  v4u ones = {ONE2, ONE2, ONE2, ONE2};
  asm volatile("" : "+v"(ones));
  v4u xf[2][16], xres;
  const f4 z4 = {0, 0, 0, 0};
  f4 ak[2] = {z4, z4}, av[2] = {z4, z4}, cs = z4;
  const int xrd = i * 128 + (g ^ (i >> 1)) * 16;
  LAS char* xp[2][2][2];
#pragma unroll
  for (int o = 0; o < 2; ++o)
#pragma unroll
    for (int h = 0; h < 2; ++h) { xp[o][0][h] = L + L_KVRED + (w ^ o) * 2048 + (xrd ^ (h * 64)); xp[o][1][h] = L + L_WO + (w ^ o) * 4096 + (xrd ^ (h * 64)); }
#define XRD(o, c, h) xf[o][2 * (c) + (h)] = ldf((c) % 3 ? xp[o][1][h] + ((c) % 3 - 1) * 2048 : xp[o][0][h])
  v4u wf[2][4], cf, xs, pr1, pro;
  f4 prc;
  f4 mr[2], rs[2];
  f2 c2k, c2v;
  const int cres = ((j >> 1) - rot) & 7;
  const int rot4 = (j & 3) * 4;
#define P1L(N, slot, n) if (!(XP & 2)) dmac<N>(dWS, lane * 16, WS_P1 + (j * 112 + (((slot) + rot4) & 15) * 7 + (n)) * 1024, lds0 + L_W1 + ((slot) * 7 + (n)) * 1024)
#pragma unroll
  for (int c = -1; c < 8; ++c) {
    if (c >= 0) {
      ak[0] = mfma32(xf[0][2 * c], wf[c & 1][0], ak[0]);
      ak[1] = mfma32(xf[1][2 * c], wf[c & 1][0], ak[1]);
      SB();
    }
    if (c == -1) asm volatile("s_waitcnt vmcnt(3)" : "+v"(xrow), "+v"(tag), "+v"(tch) :: "memory");
    else if (c < 6) VMWAIT(c < 2 ? 6 : 3);
    else if (c == 6) {
      asm volatile("s_waitcnt vmcnt(2)" : "+v"(xs) :: "memory");
#pragma nounroll
      for (unsigned sp = 0; sp < SPIN && !none(xs.y != tag || xs.w != tag); ++sp) {
        COUNT(23);
        LD16(xs, dWS, i * 16, WS_XS + (p * 128 + w * 16) * 16);
        asm volatile("s_waitcnt vmcnt(0)" : "+v"(xs) :: "memory");
      }
      if (g == 0) { *(LAS unsigned*)(L + L_STT + w * 128 + i * 4) = xs.x; *(LAS unsigned*)(L + L_STT + w * 128 + 64 + i * 4) = xs.z; }
    } else LGKM0();
    SBAR();
    if (c >= 0) {
      av[0] = mfma32(xf[0][2 * c], wf[c & 1][1], av[0]);
      av[1] = mfma32(xf[1][2 * c], wf[c & 1][1], av[1]);
    }
    if (c < 7) {
      XRD(0, c + 1, 0); XRD(1, c + 1, 0); XRD(0, c + 1, 1); XRD(1, c + 1, 1);
      cf = trfrag(kC + (c + 1) * 4096, 256);
    } else {
#pragma unroll
      for (int o = 0; o < 2; ++o) { mr[o] = *(LAS f4*)(L + L_STT + (w ^ o) * 128 + g * 16); rs[o] = *(LAS f4*)(L + L_STT + (w ^ o) * 128 + 64 + g * 16); }
      c2k = *(LAS f2*)(L + L_CKV + (hd * 16 + i) * 8); c2v = *(LAS f2*)(L + L_CKV + (32 + hd * 16 + i) * 8);
    }
    if (c < 5) WLOAD(c + 3);
    if (c == 5) LD16(xs, dWS, i * 16, WS_XS + (p * 128 + w * 16) * 16);
    SB();
    if (c >= 0) {
      ak[0] = mfma32(xf[0][2 * c + 1], wf[c & 1][2], ak[0]);
      ak[1] = mfma32(xf[1][2 * c + 1], wf[c & 1][2], ak[1]);
    }
    if (c < 7) { wf[(c + 1) & 1][0] = WFRAG(2 * c + 2, 0); wf[(c + 1) & 1][1] = WFRAG(2 * c + 2, 1); }
    if (c < 5) XLOAD1(c + 3, 0); else if (c < 7) QLOAD1(2 * c - 10);
    SB();
    if (c >= 0) {
      av[0] = mfma32(xf[0][2 * c + 1], wf[c & 1][3], av[0]);
      av[1] = mfma32(xf[1][2 * c + 1], wf[c & 1][3], av[1]);
    }
    if (c < 7) { wf[(c + 1) & 1][2] = WFRAG(2 * c + 3, 0); wf[(c + 1) & 1][3] = WFRAG(2 * c + 3, 1); }
    if (c < 5) XLOAD1(c + 3, 1); else if (c < 7) QLOAD1(2 * c - 9);
    if (c == 7) {
      st16(pr1, rWS, lane * 16, w < 7 ? WS_P1 + ((j * 16 + p) * 7 + pos1) * 1024 : WS_BYTES);
      if (i == 0) st16(__builtin_bit_cast(v4u, prc), rWS, g * 16, w < 7 ? WS_PC + ((j * 7 + pos1) * 16 + p) * 64 : WS_BYTES);
      if (w >= 6) st16(pro, rWS, lane * 16, WS_PO + ((j * 16 + p) * 2 + (w & 1)) * 1024);
    }
    SB();
    if (c < 7) {
      cs = mfma32(ones, cf, cs);
      if (c + 1 == cres) xres = (j & 1) ? xf[0][2 * c + 3] : xf[0][2 * c + 2];
    }
    if (c == -1) {
      rawload();
  tag = __builtin_amdgcn_readfirstlane(tag) + 1u;
  {
    float s = dot2(xrow.x, ONE2, dot2(xrow.y, ONE2, 0.f)) + dot2(xrow.z, ONE2, dot2(xrow.w, ONE2, 0.f));
    float ss = dot2(xrow.x, xrow.x, dot2(xrow.y, xrow.y, 0.f)) + dot2(xrow.z, xrow.z, dot2(xrow.w, xrow.w, 0.f));
    s = rowsum4(dppadd<0x128>(dppadd<0x124>(dppadd<0x122>(dppadd<0x121>(s))))) * (1.f / 512.f);
    ss = rowsum4(dppadd<0x128>(dppadd<0x124>(dppadd<0x122>(dppadd<0x121>(ss))))) * (1.f / 512.f);
    const float r = __builtin_amdgcn_rsqf(fmaxf(ss - s * s, 0.f) + 1e-5f);
    if (lane == 0) st16(v4u{asu(s), tag, asu(r), tag}, rWS, 0, WS_XS + (p * 128 + j * 8 + w) * 16);
  }
    }
    if (c == RAWAT + 3) {
      const int il = (pp * 2 + (w & 1)) * 8;
      pr1 = trfrag(L + L_RAW + (8 * g + q) * 224 + pp * 8 + w * 32, 4 * 224);
      pro = trfrag(L + L_RAW + 7168 + (8 * g + q) * 64 + il, 256);
      asm volatile("" :: "v"(tch));
    }
    if (c == RAWAT + 4) prc = mfma32(pr1, ones, z4);
    if (c == 6 && g == 0) *(LAS float*)(L + L_CKV + ((w & 3) * 16 + i) * 8 + (w >> 2) * 4) = cs[0];
  }
  STAMP(3);
  v4u w2r[28];
  const __amdgpu_buffer_rsrc_t rW2 = __builtin_amdgcn_make_buffer_rsrc((void*)w2, 0, 1792 * 512 * 2, 0x00020000);
  const int vW2 = (((lane >> 3) & 1) * 16 + g * 4) * 1024 + (lane & 7) * 16, vW2n = (((lane >> 3) & 1) * 4 + g * 8) * 1024 + (lane & 7) * 16, sW2 = 224 * w * 1024 + c4 * 128;
#define W2L(n) w2r[n] = __builtin_amdgcn_raw_buffer_load_b128(rW2, (((n) >> 2) == 3 ? vW2n : vW2) + ((n) & 3) * 1024, sW2 + ((n) >> 2) * 32768, 0)
  {
    const float ck = c2k[0] + c2k[1], cv = c2v[0] + c2v[1];
    float kk[8], vv[8];
#pragma unroll
    for (int n = 0; n < 8; ++n) {
      kk[n] = phi((ak[n >> 2][n & 3] - mr[n >> 2][n & 3] * ck) * rs[n >> 2][n & 3]);
      vv[n] = (av[n >> 2][n & 3] - mr[n >> 2][n & 3] * cv) * rs[n >> 2][n & 3];
    }
    const v4u pkf = {pk(kk[0], kk[1]), pk(kk[2], kk[3]), pk(kk[4], kk[5]), pk(kk[6], kk[7])};
    const v4u pvf = {pk(vv[0], vv[1]), pk(vv[2], vv[3]), pk(vv[4], vv[5]), pk(vv[6], vv[7])};
    const f4 kv = mfma32(pkf, pvf, z4), z = mfma32(pkf, ones, z4);
    *(LAS f4*)(Ll + L_KVRED + w * 1024) = kv;
    if (i == 0) *(LAS f4*)(L + L_ZP + w * 64 + g * 16) = z;
  }
  if (w < 6) VMWAIT(2); else VMWAIT(3);
  BAR();
  STAMP(4);
  if (w < 2) {
    __builtin_amdgcn_s_setprio(3);
    f4 t[8];
#pragma unroll
    for (int n = 0; n < 4; ++n) { t[n] = *(LAS f4*)(Ll + L_KVRED + (n * 2 + w) * 1024); t[4 + n] = *(LAS f4*)(L + L_ZP + (n * 2 + w) * 64 + pp * 16); }
    SB();
    const int so = WS_KV + ((j * 2 + w) * 16 + p) * KV_REC;
    const f4 a = (t[0] + t[1]) + (t[2] + t[3]), z = (t[4] + t[5]) + (t[6] + t[7]);
    st16(v4u{pk(a[0], a[1]), tag, pk(a[2], a[3]), tag}, rWS, lane * 16, so);
    if (lane < 8) st16(v4u{pk(z[0], z[1]), tag, pk(z[2], z[3]), tag}, rWS, lane * 16 + 1024, so);
    STAMP(2);
  }
  v2u pq[2];
  v4u kt[5];
  const unsigned kso = WS_KV + ((j * 2 + hd) * 16 + 4 * (w >> 1)) * KV_REC;
  {
    f4 aq[2] = {z4, z4}, cq = z4;
    v4u qf[4];
    float m1[2], r1[2];
    f4 cp[4];
#pragma unroll
    for (int s = 0; s < 3; ++s) qf[s] = trfrag(tN + L_WQ + s * 2048, 256);
    {
      v4u t[4];
#pragma unroll
      for (int n = 0; n < 4; ++n) t[n] = trfrag(tN + L_WQ + (w >> 1) * 8192 + n * 2048, 256);
#pragma unroll
      for (int n = 0; n < 4; ++n) cq = mfma32(t[n], ones, cq);
    }
#pragma unroll
    for (int s = 0; s < 16; ++s) {
      if (s + 3 < 16) qf[(s + 3) & 3] = trfrag(tN + L_WQ + (s + 3) * 2048, 256);
      aq[0] = mfma32(qf[s & 3], xf[0][s], aq[0]);
      aq[1] = mfma32(qf[s & 3], xf[1][s], aq[1]);
      if (s == 1 && i == 0) *(LAS f4*)(L + L_CQ + w * 64 + g * 16) = cq;
      if (s >= 2 && s < 2 + W2Q) W2L(s - 2);
      if (s == 13) {
#pragma unroll
        for (int o = 0; o < 2; ++o) { m1[o] = *(LAS float*)(L + L_STT + (w ^ o) * 128 + i * 4); r1[o] = *(LAS float*)(L + L_STT + (w ^ o) * 128 + 64 + i * 4); }
      }
      if (s == 14) {
        VMWAIT(5 + ((XP & 1) ? 0 : W2Q));
        __builtin_amdgcn_s_setprio(0);
        SBAR();
#pragma unroll
        for (int n = 0; n < 4; ++n) cp[n] = *(LAS f4*)(L + L_CQ + (2 * n + hd) * 64 + g * 16);
      }
      if (s == KVAT) {
#pragma unroll
        for (int n = 0; n < 4; ++n) LD16(kt[n], dWS, lane * 16, kso + n * KV_REC);
        LD16(kt[4], dWS, q * KV_REC + pp * 16 + 1024, kso);
      }
      SB();
    }
#pragma unroll
    for (int n = W2Q; n < W2Q + W2R; ++n) W2L(n);
    SB();
    if (tid == 128) __hip_atomic_store(myflag, tag, RLX_AGENT);
    cq = (cp[0] + cp[1]) + (cp[2] + cp[3]);
#pragma unroll
    for (int o = 0; o < 2; ++o) {
      const float m = m1[o], r = r1[o];
      float a[4];
#pragma unroll
      for (int n = 0; n < 4; ++n) a[n] = phi((aq[o][n] - m * cq[n]) * r);
      pq[o] = v2u{pk(a[0], a[1]), pk(a[2], a[3])};
    }
    asm volatile("" : "+v"(pq[0]), "+v"(pq[1]));
  }
  STAMP(5);
  {
    asm volatile("s_waitcnt vmcnt(%5)" : "+v"(kt[0]), "+v"(kt[1]), "+v"(kt[2]), "+v"(kt[3]), "+v"(kt[4]) : "i"((XP & 1) ? 0 : W2R) : "memory");
#pragma nounroll
    for (unsigned sp = 0; sp < SPIN; ++sp) {
      unsigned bad = 0;
#pragma unroll
      for (int n = 0; n < 5; ++n) bad |= (kt[n].y ^ tag) | (kt[n].w ^ tag);
      if (none(bad != 0)) break;
      COUNT(24);
#pragma unroll
      for (int n = 0; n < 4; ++n) LD16(kt[n], dWS, lane * 16, kso + n * KV_REC);
      LD16(kt[4], dWS, q * KV_REC + pp * 16 + 1024, kso);
      asm volatile("s_waitcnt vmcnt(0)" : "+v"(kt[0]), "+v"(kt[1]), "+v"(kt[2]), "+v"(kt[3]), "+v"(kt[4]) :: "memory");
    }
    STAMP(6);
    const f4 s = {(bflo(kt[0].x) + bflo(kt[1].x)) + (bflo(kt[2].x) + bflo(kt[3].x)), (bfhi(kt[0].x) + bfhi(kt[1].x)) + (bfhi(kt[2].x) + bfhi(kt[3].x)),
                  (bflo(kt[0].z) + bflo(kt[1].z)) + (bflo(kt[2].z) + bflo(kt[3].z)), (bfhi(kt[0].z) + bfhi(kt[1].z)) + (bfhi(kt[2].z) + bfhi(kt[3].z))};
    *(LAS f4*)(Ll + L_CONS + w * 1024) = s;
    const f4 z = {dppadd<0x128>(dppadd<0x124>(bflo(kt[4].x))), dppadd<0x128>(dppadd<0x124>(bfhi(kt[4].x))),
                  dppadd<0x128>(dppadd<0x124>(bflo(kt[4].z))), dppadd<0x128>(dppadd<0x124>(bfhi(kt[4].z)))};
    if (lane < 4) *(LAS f4*)(L + L_ZC + w * 64 + lane * 16) = z;
  }
#pragma unroll
  for (int n = W2Q + W2R; n < W2Q + W2R + W2S; ++n) W2L(n);
  BAR();
  STAMP(7);
  unsigned pf = tag;
  if (w == 0) asm volatile("buffer_load_dword %0, %1, %2, %3 offen sc1" : "+v"(pf) : "v"((i * 16 + j) * 128), "s"(dWS), "s"(WS_FLAG) : "memory");
  const int ohoff = ((p * 8 + w) * 16 + j) * 1024;
  {
    f4 kq[4], zq[4];
#pragma unroll
    for (int n = 0; n < 4; ++n) { kq[n] = *(LAS f4*)(Ll + L_CONS + (2 * n + hd) * 1024); zq[n] = *(LAS f4*)(L + L_ZC + (2 * n + hd) * 64 + g * 16); }
    SB();
    const f4 k0 = (kq[0] + kq[1]) + (kq[2] + kq[3]), z0 = (zq[0] + zq[1]) + (zq[2] + zq[3]);
    const v2u ka = {pk(k0[0], k0[1]), pk(k0[2], k0[3])}, za = {pk(z0[0], z0[1]), pk(z0[2], z0[3])};
    const f4 n0 = mfma16(ka, pq[0], z4), n1 = mfma16(ka, pq[1], z4), d0 = mfma16(za, pq[0], z4), d1 = mfma16(za, pq[1], z4);
    const float r0 = __builtin_amdgcn_rcpf(d0[0] + 1e-6f), r1 = __builtin_amdgcn_rcpf(d1[0] + 1e-6f);
    const auto sx = __builtin_amdgcn_permlane16_swap(pk(n0[0] * r0, n0[1] * r0), pk(n1[0] * r1, n1[1] * r1), false, false);
    const auto sy = __builtin_amdgcn_permlane16_swap(pk(n0[2] * r0, n0[3] * r0), pk(n1[2] * r1, n1[3] * r1), false, false);
    st16(v4u{sx[0], sy[0], sx[1], sy[1]}, rWS, ((hd ^ g) & 1) * 16384 + ((2 * hd + (g >> 1)) * 16 + i) * 16, WS_O + ohoff - hd * 16384);
  }
  asm volatile("s_waitcnt vmcnt(0)" : "+v"(pf) :: "memory");
  if (!none(pf != tag)) poll(flags + (i * 16 + j) * 32, tag, 16, lane);
  SBAR();
  if (tid == 0) __hip_atomic_store(myflag + 256 * 32, tag, RLX_AGENT);
  STAMP(20);
  {
    const v4u ct = __builtin_amdgcn_raw_buffer_load_b128(rWS, lane * 16, WS_PC + (j * 7 + (w ? w - 1 : 0)) * 1024, SC1);
    const unsigned bias = __builtin_amdgcn_raw_buffer_load_b16(__builtin_amdgcn_make_buffer_rsrc((void*)(b1 + 112 * j), 0, 224, 0x00020000), (((w - 1 + (j & 1)) % 7) * 16 + pp * 4 + q) * 2, 0, 0);
    if (!(XP & 2)) dmac<4>(dWS, lane * 16, WS_PO + (j * 32 + ((2 * w + rot4) & 15) * 2) * 1024, lds0 + L_WO + w * 4096);
    P1L(4, 2 * w, 0);
    w2pack(w2r); SB();
    if (w) {
      const f4 c = {rowsum4(dppadd<0x128>(dppadd<0x124>(asf(ct.x)))), rowsum4(dppadd<0x128>(dppadd<0x124>(asf(ct.y)))),
                    rowsum4(dppadd<0x128>(dppadd<0x124>(asf(ct.z)))), rowsum4(dppadd<0x128>(dppadd<0x124>(asf(ct.w))))};
      const float cf = q == 0 ? c[0] : q == 1 ? c[1] : q == 2 ? c[2] : c[3];
      const unsigned ch = pk(cf, cf), cl = pk(cf - bflo(ch), 0.f);
      if (g == 0) *(LAS v4u*)(L + L_C1 + ((w - 1) * 16 + pp * 4 + q) * 16) = v4u{ch, (cl & 0xffffu) | (bias << 16), bias, 0u};
    } else poll(flags + (256 + p * 16 + i) * 32, tag, 16, lane);
  }
  VMWAIT(0);
  SBAR();
  STAMP(8);
#define LOAD1(s) f[s] = __builtin_amdgcn_raw_buffer_load_b128(rWS, lane * 16 + ((s) & 3) * 1024, so + ((((s) & 12) + rot4) & 15) * 1024, SC1)
#define LOADPD() do { _Pragma("unroll") for (int s = 0; s < PD; ++s) { LOAD1(s); SB(); } } while (0)
  {
    const int so = WS_O + (p * 8 + w) * 16384;
    v4u f[16];
    LAS char* const Lo = opq(Ll + L_WO);
    LOADPD();
    f4 a0 = {bflo(xres.x), bfhi(xres.x), bflo(xres.y), bfhi(xres.y)}, a1 = {bflo(xres.z), bfhi(xres.z), bflo(xres.w), bfhi(xres.w)};
    v4u wf2[3][2];
#pragma unroll
    for (int s = 0; s < 2; ++s) { wf2[s][0] = ldf(Lo + s * 2048); wf2[s][1] = ldf(Lo + s * 2048 + 1024); }
#pragma unroll
    for (int s = 0; s < 16; ++s) {
      if (s + PD < 16) LOAD1(s + PD < 16 ? s + PD : 0);
      if (s + 2 < 16) { wf2[(s + 2) % 3][0] = ldf(Lo + (s + 2) * 2048); wf2[(s + 2) % 3][1] = ldf(Lo + (s + 2) * 2048 + 1024); }
      SB();
      a0 = mfma32(wf2[s % 3][0], f[s], a0);
      a1 = mfma32(wf2[s % 3][1], f[s], a1);
      SB();
    }
    P1L(3, 2 * w, 4);
    const v4u h = {pk(a0[0], a0[1]), pk(a0[2], a0[3]), pk(a1[0], a1[1]), pk(a1[2], a1[3])};
    st16(h, rWS, lane * 16, WS_H + ohoff);
    const f4 hs = mfma32(ones, h, z4), hg = mfma32(h, h, z4);
    if (g == q) __builtin_amdgcn_raw_buffer_store_b64(v2u{asu(hs[0]), asu(pp == 0 ? hg[0] : pp == 1 ? hg[1] : pp == 2 ? hg[2] : hg[3])}, rWS, i * 8, WS_S + (ohoff >> 3), SC1);
    if (bid == 0 && tid == 0) __builtin_amdgcn_raw_buffer_store_b32(tag, rWS, WS_EPOCH, 0, SC1);
  }
  STAMP(9);
  VMWAIT(0);
  BAR();
  if (tid == 0) __hip_atomic_store(myflag + 512 * 32, tag, RLX_AGENT);
  STAMP(21);
  P1L(4, 2 * w + 1, 0); P1L(3, 2 * w + 1, 4);
#pragma unroll
  for (int s = 1; s < (W2Q + W2R + W2S) / 4; ++s) { w2pack(w2r + 4 * s); SB(); }
  v4u cb[7];
#pragma unroll
  for (int t = 0; t < 7; ++t) cb[t] = ldf(L + L_C1 + (t * 16 + i) * 16);
  if (w == 0) poll(flags + (512 + p * 16 + i) * 32, tag, 16, lane);
  VMWAIT(0);
  SBAR();
  STAMP(10);
  v4u hres;
  constexpr int W2E = W2Q + W2R + W2S, W2B = W2E + W2P;
  {
    v4u f[16];
    const int so = WS_H + (p * 8 + w) * 16384;
    LAS char* const wp0 = opq(Ll + L_W1);
    LAS char* const wp1 = opq(wp0 + 8 * 7168);
#define W1F(n) ldf(((n) < 56 ? wp0 : wp1) + ((n) % 56) * 1024)
    f4 acc[7];
    if (PRIO3 == 1 || (PRIO3 == 2 && w >= 4)) __builtin_amdgcn_s_setprio(2);
    v2u st[4];
#pragma unroll
    for (int n = 0; n < 4; ++n) st[n] = __builtin_amdgcn_raw_buffer_load_b64(rWS, g * 512 + i * 8 + n * 128, WS_S + (p * 8 + w) * 2048, SC1);
    SB();
    LOADPD();
    float rstd2;
    {
      v4u wf3[6];
#pragma unroll
      for (int n = 0; n < 4; ++n) wf3[n] = W1F(n);
      SB();
      {
        const float mean2 = rowsum4((asf(st[0].x) + asf(st[1].x)) + (asf(st[2].x) + asf(st[3].x))) * (1.f / 512.f);
        const float var = fmaxf(rowsum4((asf(st[0].y) + asf(st[1].y)) + (asf(st[2].y) + asf(st[3].y))) * (1.f / 512.f) - mean2 * mean2, 0.f) + 1e-5f;
        rstd2 = __builtin_amdgcn_rsqf(var);
        const float sig = var * rstd2;
        const unsigned msk = g ? 0u : ~0u;
        const unsigned hi = pk(-mean2, sig), lo = pk(-mean2 - bflo(hi), sig - bfhi(hi));
        const v4u ex = {((hi & 0xffffu) | (lo << 16)) & msk, hi & msk, (lo >> 16) & msk, 0u};
#pragma unroll
        for (int t = 0; t < 7; ++t) acc[t] = mfma32(cb[t], ex, z4);
      }
      SFOR(n, 0, 112, 2)
        if (n + 4 < 112) { wf3[(n + 4) % 6] = W1F(n + 4); wf3[(n + 5) % 6] = W1F(n + 5); }
        SB();
        acc[(n + 1) % 7] = mfma32(wf3[(n + 1) % 6], f[(n + 1) / 7], acc[(n + 1) % 7]);
        acc[n % 7] = mfma32(wf3[n % 6], f[n / 7], acc[n % 7]);
        constexpr int k = n / 4 + PD;
        if ((n & 2) == 0) {
          if (k < 16) LOAD1(k < 16 ? k : 0);
          else if (k == 16) hres = __builtin_amdgcn_raw_buffer_load_b128(rWS, lane * 16, WS_H + ((p * 8 + 4 * th + (w >> 1)) * 16 + 2 * c4 + hd) * 1024, SC1);
          else if (W2E + k - 17 < W2B) W2L(k > 16 && W2E + k - 17 < W2B ? W2E + k - 17 : 0);
        }
        SB();
      SEND
    }
    if (PRIO3) __builtin_amdgcn_s_setprio(0);
    STAMP(11);
    v2u e[7];
#define SINGLE(v, off) do { const auto a_ = __builtin_amdgcn_permlane16_swap(v.x, v.x, false, false), b_ = __builtin_amdgcn_permlane16_swap(v.y, v.y, false, false); \
      if ((g & 1) == 0) st16(v4u{a_[0], b_[0], a_[1], b_[1]}, rWS, (g >> 1) * 256 + i * 16, aoff + (off)); } while (0)
    const int aoff = WS_A + ((p * 8 + w) * 56 + ((7 * j + 1) >> 1)) * 1024;
#pragma unroll
    for (int t = 0; t < 7; ++t) {
      const f4 a = acc[t] * (rstd2 * LOG2E);
      const f4 m = f4{__builtin_amdgcn_exp2f(a[0]), __builtin_amdgcn_exp2f(a[1]), __builtin_amdgcn_exp2f(a[2]), __builtin_amdgcn_exp2f(a[3])} + 1.f;
      const f4 d = m * m + 1.f;
      const f4 y = acc[t] * (f4{__builtin_amdgcn_rcpf(d[0]), __builtin_amdgcn_rcpf(d[1]), __builtin_amdgcn_rcpf(d[2]), __builtin_amdgcn_rcpf(d[3])} * (-2.f * rstd2) + rstd2);
      e[t] = v2u{pk(y[0], y[1]), pk(y[2], y[3])};
      if (t & 1) { st16(v4u{e[t - 1].x, e[t - 1].y, e[t].x, e[t].y}, rWS, lane * 16 + (t >> 1) * 1024, aoff); SB(); }
    }
    SINGLE(e[6], (j & 1) ? -512 : 3072);
  }
  STAMP(13);
#pragma unroll
  for (int n = W2B; n < W2B + W2X; ++n) W2L(n);
  VMWAIT((XP & 1) ? 0 : W2X);
  BAR();
  if (tid == 0) __hip_atomic_store(myflag + 768 * 32, tag, RLX_AGENT);
  STAMP(14);
#pragma unroll
  for (int n = W2B + W2X; n < 28; ++n) W2L(n);
  SB();
  *(LAS v4u*)(L + L_HRES + w * 1024 + (16 * g + (i ^ (4 * g))) * 16) = hres;
#pragma unroll
  for (int s = W2E / 4; s < (W2B + W2X) / 4; ++s) { w2pack(w2r + 4 * s); SB(); }
  poll(flags + (768 + p * 16 + 2 * w + (lane & 1)) * 32, tag, 2, lane);
  STAMP(15);
  {
    const int so = WS_A + ((p * 8 + 4 * th) * 56 + 7 * w) * 1024;
    constexpr int NB = NB4;
    if (PRIO4 && w >= 4) __builtin_amdgcn_s_setprio(1);
    const int rt = j & 3;
    v4u f[NB];
#define ALOAD(n) __builtin_amdgcn_raw_buffer_load_b128(rWS, lane * 16 + (((n) % 7) & 3) * 1024, so + (((((n) / 7) + rt) & 3) * 56 + (((n) % 7) & 4)) * 1024, SC1)
#pragma unroll
    for (int n = 0; n < NB; ++n) { f[n] = ALOAD(n); SB(); }
#pragma unroll
    for (int s = (W2B + W2X) / 4; s < 7; ++s) { w2pack(w2r + 4 * s); SB(); }
    const int wr = (w * 1024 + 16 * g + (i ^ ((g & 1) << 3))) * 16;
    f4 acc[4];
#pragma unroll
    for (int n = 0; n < 28; ++n) {
      const int s = n % 7, t = n / 7;
#pragma unroll
      for (int u = 0; u < 4; ++u) acc[u] = mfma32(w2r[s * 4 + u], f[n % NB], s ? acc[u] : z4);
      if (n + NB < 28) f[n % NB] = ALOAD(n + NB < 28 ? n + NB : 0);
      if (s == 6) {
#pragma unroll
        for (int r = 0; r < 4; ++r) *(LAS f4*)(L + (wr ^ (r << 5)) + ((t + rt) & 3) * 4096 + r * 1024) = f4{acc[0][r], acc[1][r], acc[2][r], acc[3][r]};
      }
      SB();
    }
    STAMP(16);
    BAR();
    STAMP(17);
    const int tk = hd * 8 + (lane >> 3);
    LAS char* const rp = opq(L + (w >> 1) * 4096 + pp * 1024 + (16 * (q & 1) + (tk ^ ((q & 1) << 3) ^ (pp << 1))) * 16);
    LAS char* const rq = opq(rp + 65536);
    hres = *(LAS v4u*)(L + L_HRES + ((w & 6) + (q & 1)) * 1024 + (16 * pp + (tk ^ (4 * pp))) * 16);
    f4 t[16];
#pragma unroll
    for (int n = 0; n < 16; ++n) t[n] = *(LAS f4*)((n & 4 ? rq : rp) + (n & 3) * 16384 + (n >> 3) * 512);
    SB();
    const f4 a0 = ((t[0] + t[1]) + (t[2] + t[3])) + ((t[4] + t[5]) + (t[6] + t[7]));
    const f4 a1 = ((t[8] + t[9]) + (t[10] + t[11])) + ((t[12] + t[13]) + (t[14] + t[15]));
    const v4u o = {pk(a0[0] + bflo(hres.x), a0[1] + bfhi(hres.x)), pk(a0[2] + bflo(hres.y), a0[3] + bfhi(hres.y)),
                   pk(a1[0] + bflo(hres.z), a1[1] + bfhi(hres.z)), pk(a1[2] + bflo(hres.w), a1[3] + bfhi(hres.w))};
    __builtin_amdgcn_raw_buffer_store_b128(o, __builtin_amdgcn_make_buffer_rsrc(out, 0, 2048 * 512 * 2, 0x00020000), ((p * 128 + th * 64 + w * 8 + (lane >> 3)) * 512 + 64 * c4 + 8 * (lane & 7)) * 2, 0, OUTAUX);
  }
  STAMP(18);
  STAMP_FLUSH
}

extern "C" void kernel_launch(void* const* d_in, const int* in_sizes, int n_in, void* d_out, int out_size, void* d_ws, size_t ws_size,
                              hipStream_t stream) {
  (void)in_sizes; (void)n_in; (void)out_size; (void)ws_size;
  (void)hipFuncSetAttribute((const void*)fused_block, hipFuncAttributeMaxDynamicSharedMemorySize, LDS_BYTES);
  fused_block<<<256, 512, LDS_BYTES, stream>>>((const u16*)d_in[0], (const u16*)d_in[1], (const u16*)d_in[2], (const u16*)d_in[4],
                                               (const u16*)d_in[5], (const u16*)d_in[6], (u16*)d_out, (char*)d_ws);
}
```

```cpp
#include <hip/hip_runtime.h>
#include <hip/hip_bf16.h>

typedef unsigned short u16;
typedef unsigned v4u __attribute__((ext_vector_type(4)));
typedef unsigned v2u __attribute__((ext_vector_type(2)));
typedef float f4 __attribute__((ext_vector_type(4)));
typedef float f2 __attribute__((ext_vector_type(2)));
typedef short s4 __attribute__((ext_vector_type(4)));
typedef __bf16 b8 __attribute__((ext_vector_type(8)));
typedef __bf16 b2 __attribute__((ext_vector_type(2)));
#define GAS __attribute__((address_space(1)))
#define LAS __attribute__((address_space(3)))
#define NOPK 1
#if defined(__HIP_DEVICE_COMPILE__) && NOPK
#define TGT __attribute__((target("no-packed-fp32-ops")))
#else
#define TGT
#endif
#define DI __device__ __forceinline__ TGT
#define RLX_AGENT __ATOMIC_RELAXED, __HIP_MEMORY_SCOPE_AGENT

#define MAPMODE 1
#define XP 0
#define OUTAUX 17
#define RAWPOL "nt"
#define RAWAT -1
#define CODE_KB 22
#define NOB 0
#define NB4 5
#define PRIO4 0
#define W2Q 4
#define W2R 4
#define W2S 4
#define PRIO3 1
#define W2P 8
#define W2X 4
#define G2A 4
#define P2T 4
#define ROTM 1
#define ROTX 0
#define W2T 4
#define RT4 0
#define RT3 0
#define DEFER 1
#define NA 4
#define PD 6
#define KVAT 15
#define W2A 4

constexpr int WS_EPOCH = 0;
constexpr int WS_FLAG = 4096;
constexpr int WS_XS = 196608;
constexpr int WS_KV = 262144;
constexpr int KV_REC = 1152;
constexpr int WS_O = 1 << 20;
constexpr int WS_H = 3 << 20;
constexpr int WS_A = 5 << 20;
constexpr int WS_S = 12 << 20;
constexpr int WS_P1 = 13 << 20;
constexpr int WS_PO = 15 << 20;
constexpr int WS_PC = WS_PO + (1 << 19);
constexpr int WS_BYTES = 16 << 20;
#define STAMP(n)
#define STAMP_DECL
#define STAMP_FLUSH
#define COUNT(n)

constexpr int L_WQ = 0, L_WK = 32768, L_KVRED = 98304, L_WO = 114688, L_SCR = 147456;
constexpr int L_RAW = 0;
constexpr int L_W1 = 0;
constexpr int L_CONS = L_SCR;
constexpr int L_ZP = L_SCR + 8192;
constexpr int L_ZC = L_ZP + 512;
constexpr int L_CKV = L_ZC + 512;
constexpr int L_STT = L_CKV + 512;
constexpr int L_C1 = L_STT + 1024;
constexpr int L_CQ = L_C1 + 2048;
constexpr int L_WO2 = 81920, L_EX = 57344;
constexpr int L_HRES = 131072;
constexpr int LDS_BYTES = 163840;

constexpr unsigned ONE2 = 0x3F803F80u;
constexpr int SC1 = 16;
constexpr float LOG2E = 1.4426950408889634f;
constexpr unsigned SPIN = 1u << 15;

DI float asf(unsigned u) { return __builtin_bit_cast(float, u); }
DI unsigned asu(float f) { return __builtin_bit_cast(unsigned, f); }
DI f4 mfma32(v4u a, v4u b, f4 c) { return __builtin_amdgcn_mfma_f32_16x16x32_bf16(__builtin_bit_cast(b8, a), __builtin_bit_cast(b8, b), c, 0, 0, 0); }
DI f4 mfma16(v2u a, v2u b, f4 c) { return __builtin_amdgcn_mfma_f32_16x16x16bf16_1k(__builtin_bit_cast(s4, a), __builtin_bit_cast(s4, b), c, 0, 0, 0); }
DI unsigned pk(float lo, float hi) { f2 v = {lo, hi}; return __builtin_bit_cast(unsigned, __builtin_convertvector(v, b2)); }
DI float bflo(unsigned u) { return asf(u << 16); }
DI float bfhi(unsigned u) { return asf(u & 0xffff0000u); }
DI v2u trd(LAS char* p) { return __builtin_bit_cast(v2u, __builtin_amdgcn_ds_read_tr16_b64_v4i16((LAS s4*)p)); }
DI v4u trfrag(LAS char* p, int second) { v2u a = trd(p), b = trd(p + second); return v4u{a.x, a.y, b.x, b.y}; }
DI v4u ldf(LAS char* p) { return *(LAS v4u*)p; }
DI float dot2(unsigned a, unsigned b, float c) { return __builtin_amdgcn_fdot2_f32_bf16(__builtin_bit_cast(b2, a), __builtin_bit_cast(b2, b), c, false); }
template <int C> DI float dppadd(float v) { return v + asf(__builtin_amdgcn_mov_dpp(asu(v), C, 0xf, 0xf, true)); }
DI float rowsum4(float v) {
  auto r = __builtin_amdgcn_permlane32_swap(asu(v), asu(v), false, false);
  const float s = asf(r[0]) + asf(r[1]);
  auto t = __builtin_amdgcn_permlane16_swap(asu(s), asu(s), false, false);
  return asf(t[0]) + asf(t[1]);
}
DI bool none(bool c) { return __builtin_amdgcn_ballot_w64(c) == 0; }
DI v4u mksrd(const void* p, unsigned bytes) {
  unsigned long long a = (unsigned long long)p;
  return v4u{(unsigned)a, (unsigned)(a >> 32) & 0xffffu, bytes, 0x00020000u};
}
DI void dma16(v4u srd, unsigned voff, unsigned soff, unsigned lds) {
  asm volatile("s_mov_b32 m0, %3\n\ts_nop 0\n\tbuffer_load_dwordx4 %0, %1, %2 offen lds" ::"v"(voff), "s"(srd), "s"(soff), "s"(lds) : "memory");
}
DI void dma16nt(v4u srd, unsigned voff, unsigned soff, unsigned lds) {
  asm volatile("s_mov_b32 m0, %3\n\ts_nop 0\n\tbuffer_load_dwordx4 %0, %1, %2 offen " RAWPOL " lds" ::"v"(voff), "s"(srd), "s"(soff), "s"(lds) : "memory");
}
template <int N> DI void dmac(v4u srd, unsigned voff, unsigned so, unsigned lds) {
  if (N == 1) asm volatile("s_mov_b32 m0, %3\n\ts_nop 0\n\tbuffer_load_dwordx4 %0, %1, %2 offen sc1 lds" ::"v"(voff), "s"(srd), "s"(so), "s"(lds) : "memory");
  if (N == 3) asm volatile("s_mov_b32 m0, %3\n\ts_nop 0\n\tbuffer_load_dwordx4 %0, %1, %2 offen sc1 lds\n\tbuffer_load_dwordx4 %0, %1, %2 offen offset:1024 sc1 lds\n\t"
                           "buffer_load_dwordx4 %0, %1, %2 offen offset:2048 sc1 lds" ::"v"(voff), "s"(srd), "s"(so), "s"(lds) : "memory");
  if (N == 4) asm volatile("s_mov_b32 m0, %3\n\ts_nop 0\n\tbuffer_load_dwordx4 %0, %1, %2 offen sc1 lds\n\tbuffer_load_dwordx4 %0, %1, %2 offen offset:1024 sc1 lds\n\t"
                           "buffer_load_dwordx4 %0, %1, %2 offen offset:2048 sc1 lds\n\tbuffer_load_dwordx4 %0, %1, %2 offen offset:3072 sc1 lds"
                           ::"v"(voff), "s"(srd), "s"(so), "s"(lds) : "memory");
}
#define LD16(dst, srd, voff, soff) asm volatile("buffer_load_dwordx4 %0, %1, %2, %3 offen sc1" : "=v"(dst) : "v"(voff), "s"(srd), "s"(soff) : "memory")
template <int OFF> DI void ld16i(v4u& d, v4u srd, unsigned voff, unsigned soff) {
  asm volatile("buffer_load_dwordx4 %0, %1, %2, %3 offen offset:%4 sc1" : "=v"(d) : "v"(voff), "s"(srd), "s"(soff), "i"(OFF) : "memory");
}
template <int N> DI void vmpin(v4u& d) { asm volatile("s_waitcnt vmcnt(%1)" : "+v"(d) : "i"(N)); }
DI void st16(v4u v, __amdgpu_buffer_rsrc_t r, int voff, int soff) {
  __builtin_amdgcn_raw_buffer_store_b128(v, r, voff, soff, SC1);
  asm volatile("s_nop 1" ::"v"(v) : "memory");
}
DI LAS char* opq(LAS char* p) { asm volatile("" : "+v"(p)); return p; }
#define LGKM0() asm volatile("s_waitcnt lgkmcnt(0)" ::: "memory")
#define SBAR() do { __builtin_amdgcn_s_barrier(); asm volatile("" ::: "memory"); } while (0)
#define BAR() do { LGKM0(); SBAR(); } while (0)
#define VMWAIT(n) asm volatile("s_waitcnt vmcnt(%0)" ::"i"(n) : "memory")
#define SB() __builtin_amdgcn_sched_barrier(0)

template <int V> struct IC { static constexpr int value = V; };
template <int B, int E, int S, class F> DI void sfor(F&& f) { if constexpr (B < E) { f(IC<B>{}); sfor<B + S, E, S>(f); } }
#define SFOR(n, B, E, S) sfor<B, E, S>([&](auto n##_) __attribute__((always_inline)) TGT { constexpr int n = decltype(n##_)::value;
#define SEND });
DI float phi(float t) { return fmaxf(t, 0.f) + __builtin_amdgcn_exp2f(fminf(t, 0.f) * LOG2E) + 1e-6f; }
DI void poll(GAS unsigned* fp, unsigned tag, int n, int lane) {
#pragma nounroll
  for (unsigned sp = 0; sp < SPIN; ++sp) {
    const unsigned v = lane < n ? __hip_atomic_load(fp, RLX_AGENT) : tag;
    if (none(v != tag)) break;
  }
  asm volatile("" ::: "memory");
}

DI void w2pack(v4u* r) {
  v2u lo[4], hi[4];
#pragma unroll
  for (int n = 0; n < 4; ++n)
#pragma unroll
    for (int d = 0; d < 2; ++d) {
      const unsigned a = r[n][d], b = r[n][2 + d];
      lo[n][d] = __builtin_amdgcn_update_dpp(a, b, 0x128, 0xf, 0xc, false);
      hi[n][d] = __builtin_amdgcn_update_dpp(b, a, 0x128, 0xf, 0x3, false);
    }
#pragma unroll
  for (int u = 0; u < 4; ++u) {
    const unsigned sel = (u & 1) ? 0x07060302u : 0x05040100u;
    r[u] = v4u{__builtin_amdgcn_perm(lo[1][u >> 1], lo[0][u >> 1], sel), __builtin_amdgcn_perm(lo[3][u >> 1], lo[2][u >> 1], sel),
               __builtin_amdgcn_perm(hi[1][u >> 1], hi[0][u >> 1], sel), __builtin_amdgcn_perm(hi[3][u >> 1], hi[2][u >> 1], sel)};
  }
  asm volatile("" : "+v"(r[0]), "+v"(r[1]), "+v"(r[2]), "+v"(r[3]));
}

__global__ void __launch_bounds__(512, 2) TGT
fused_block(const u16* x_, const u16* wqkv_, const u16* wo_, const u16* w1_, const u16* b1_, const u16* w2_, u16* out_, char* ws_) {
  extern __shared__ __attribute__((aligned(16))) char smem_[];
  typedef unsigned long long u64x8 __attribute__((ext_vector_type(8)));
  u64x8 ka;
  asm volatile("s_load_dwordx16 %0, %1, 0x0" : "=s"(ka) : "s"(__builtin_amdgcn_kernarg_segment_ptr()) : "memory");
  unsigned tch = 0;
  if ((blockIdx.x >> 3) < CODE_KB && threadIdx.x < 8) {
    unsigned long long pc;
    asm volatile("s_getpc_b64 %0" : "=s"(pc));
    asm volatile("global_load_dword %0, %1, %2" : "+v"(tch) : "v"(threadIdx.x * 128), "s"((pc & ~127ull) + (blockIdx.x >> 3) * 1024) : "memory");
  }
  LAS char* L = (LAS char*)smem_;
  const int tid = threadIdx.x, lane = tid & 63, w = __builtin_amdgcn_readfirstlane(tid >> 6);
  const int g = lane >> 4, i = lane & 15, q = (lane >> 2) & 3, pp = lane & 3;
  const int bid = blockIdx.x, xcd = bid & 7, rk = bid >> 3;
  const int p = 4 * (xcd >> 1) + (rk >> 3), j = (xcd & 1) * 8 + (rk & 7);
  const int rot = ROTM == 0 ? 2 * p + j : ROTM == 1 ? p + j + ROTX * (j >> 2) : p + j;
  STAMP_DECL
  STAMP(0);
  const unsigned lds0 = (unsigned)(size_t)L;
  LAS char* const Ll = L + lane * 16;
  const int th = j >> 3, c4 = j & 7;
  const int pos1 = w < (j & 1) ? 6 : w - (j & 1);

  unsigned vW = (lane >> 2) * 3072 + (pp ^ ((lane >> 5) << 1)) * 16;
  unsigned vX0 = (lane >> 3) * 1024 + ((lane & 7) ^ g) * 16, vX1 = (vX0 ^ 64) + 8192;
  unsigned sX = (p * 128 + w * 16) * 1024;
  unsigned sW = (w & 3) * (16 * 3072) + (1 + (w >> 2)) * 1024 + j * 64, lW = lds0 + L_WK + (w >> 2) * 32768 + (w & 3) * 1024;
  const int xo = ((xcd ^ (xcd >> 1)) & 1) * 4;
#define CH(c) ((((c) & 4) ^ xo) | (((c) + rot) & 3))
  const int cres = ((c4 & 4) ^ xo) | ((c4 - rot) & 3);
#define XSLOT(c) ((c) % 3 == 0 ? L_KVRED + w * 2048 : L_WO + w * 4096 + ((c) % 3 - 1) * 2048)
#define WLOAD(c) dma16(dQKV, vW, sW + CH(c) * (64 * 3072), lW + (c) * 4096)
#define XLOAD1(c, h) dma16(dX, (h) ? vX1 : vX0, sX + CH(c) * 128, lds0 + XSLOT(c) + (h) * 1024)
#define QLOAD1(n) dma16(dQKV, vW, (CH(w) * 64 + (n) * 16) * 3072 + j * 64, lds0 + L_WQ + w * 4096 + (n) * 1024)
  unsigned to = WS_EPOCH;
  asm("v_writelane_b32 %0, %1, 1\n\tv_writelane_b32 %0, %2, 2\n\tv_writelane_b32 %0, %3, 3\n\tv_writelane_b32 %0, %4, 4" : "+v"(to)
      : "s"(WS_XS + (p * 16 + j) * 128), "s"(WS_PC + ((j * 7 + pos1) * 16 + p) * 64), "s"(WS_S + ((p * 8 + 4 * th + (w >> 1)) * 16 + 2 * c4 + (w & 1)) * 128), "s"(WS_FLAG + ((w & 3) * 256 + p * 16 + j) * 128));
  asm volatile("s_waitcnt lgkmcnt(0)" : "+s"(ka), "+v"(vW), "+v"(vX0), "+v"(vX1), "+s"(sX), "+s"(sW), "+s"(lW), "+v"(to) :: "memory");
  const u16* const x = (const u16*)ka[0]; const u16* const wqkv = (const u16*)ka[1]; const u16* const wo = (const u16*)ka[2]; const u16* const w1 = (const u16*)ka[3];
  const u16* const b1 = (const u16*)ka[4]; const u16* const w2 = (const u16*)ka[5]; u16* const out = (u16*)ka[6]; char* const ws = (char*)ka[7];
  const v4u dQKV = mksrd(wqkv, 512 * 1536 * 2), dX = mksrd(x, 2048 * 512 * 2), dWS = mksrd(ws, WS_BYTES);
  const __amdgpu_buffer_rsrc_t rWS = __builtin_amdgcn_make_buffer_rsrc(ws, 0, WS_BYTES, 0x00020000);
  WLOAD(0); XLOAD1(0, 0); XLOAD1(0, 1);
  unsigned tag;
  v4u xrow;
  asm volatile("buffer_load_dwordx4 %0, %1, %2, %3 offen" : "=v"(xrow) : "v"(lane * 16), "s"(dX), "s"((p * 128 + j * 8 + w) * 1024) : "memory");
  asm volatile("buffer_load_dword %0, %1, %2, 0 offen sc1" : "=v"(tag) : "v"(to), "s"(dWS) : "memory");
  if (w == 7) asm volatile("buffer_load_dword %0, %1, %2, %3 offen" : "+v"(tch) : "v"(((lane >> 3) & 1) * 3072 + (lane & 7) * 128), "s"(dQKV), "s"(bid * 6144) : "memory");
  SBAR();
  WLOAD(1); XLOAD1(1, 0); XLOAD1(1, 1);
  auto rawload = [&]() TGT {
    const unsigned c = w * 64 + lane, r = (c * 4682u) >> 16;
    if (w < 7) dma16nt(mksrd(w1, 512 * 1792 * 2), r * 3584 + (c - r * 14) * 16, p * (32 * 3584) + 224 * j, lds0 + L_RAW + w * 1024);
    if (w >= 6) dma16nt(mksrd(wo, 512 * 512 * 2), (lane >> 2) * 1024 + pp * 16, (p * 32 + (w & 1) * 16) * 1024 + c4 * 128 + th * 64, lds0 + L_RAW + 7168 + (w & 1) * 1024);
    asm volatile("global_load_dword %0, %1, %2" : "+v"(tch) : "v"((lane < 7 ? lane : 0) * 128), "s"((const char*)w2 + (bid * 8 + w) * 896) : "memory");
  };
  STAMP(1);
  GAS unsigned* const flags = (GAS unsigned*)(ws + WS_FLAG);
  GAS unsigned* const myflag = flags + (p * 16 + j) * 32;
  const int hd = w & 1, tile2 = 4 * th + (w >> 1);
  LAS char* const tN = L + (8 * g + q) * 64 + pp * 8 + ((g ^ hd) & 1) * 32;
  LAS char* const kN = opq(tN + L_WK);
  LAS char* const kC = opq(tN + L_WK + ((w >> 1) & 1) * 32768 + (w >> 2) * 2048);
#define WFRAG(s, m) trfrag(kN + (m) * 32768 + (s) * 2048, 256)
  v4u ones = {ONE2, ONE2, ONE2, ONE2};
  asm volatile("" : "+v"(ones));
  v4u xf[2][16], xres;
  const f4 z4 = {0, 0, 0, 0};
  f4 ak[2] = {z4, z4}, av[2] = {z4, z4}, cs = z4;
  const int xrd = i * 128 + (g ^ (i >> 1)) * 16;
  LAS char* xp[2][2][2];
#pragma unroll
  for (int o = 0; o < 2; ++o)
#pragma unroll
    for (int h = 0; h < 2; ++h) { xp[o][0][h] = L + L_KVRED + (w ^ o) * 2048 + (xrd ^ (h * 64)); xp[o][1][h] = L + L_WO + (w ^ o) * 4096 + (xrd ^ (h * 64)); }
#define XRD(o, c, h) xf[o][2 * (c) + (h)] = ldf((c) % 3 ? xp[o][1][h] + ((c) % 3 - 1) * 2048 : xp[o][0][h])
  v4u wf[2][4], cf, xs, pr1, pro;
  f4 prc;
  f4 mr[2], rs[2];
  f2 c2k, c2v;
  const int rot4 = RT3 ? (j & 3) * 4 : 0;
#define P1L(N, slot, n) if (!(XP & 2)) dmac<N>(dWS, lane * 16, WS_P1 + (j * 112 + (((slot) + rot4) & 15) * 7 + (n)) * 1024, lds0 + L_W1 + ((slot) * 7 + (n)) * 1024)
#pragma unroll
  for (int c = -1; c < 8; ++c) {
    if (c >= 0) {
      ak[0] = mfma32(xf[0][2 * c], wf[c & 1][0], ak[0]);
      ak[1] = mfma32(xf[1][2 * c], wf[c & 1][0], ak[1]);
      SB();
    }
    if (c == -1) asm volatile("s_waitcnt vmcnt(3)" : "+v"(xrow), "+v"(tag), "+v"(tch) :: "memory");
    else if (c < 6) VMWAIT(c < 2 ? 6 : 3);
    else if (c == 6) {
      asm volatile("s_waitcnt vmcnt(2)" : "+v"(xs) :: "memory");
#pragma nounroll
      for (unsigned sp = 0; sp < SPIN && !none(xs.y != tag || xs.w != tag); ++sp) {
        COUNT(23);
        LD16(xs, dWS, i * 16, WS_XS + (p * 128 + w * 16) * 16);
        asm volatile("s_waitcnt vmcnt(0)" : "+v"(xs) :: "memory");
      }
      if (g == 0) { *(LAS unsigned*)(L + L_STT + w * 128 + i * 4) = xs.x; *(LAS unsigned*)(L + L_STT + w * 128 + 64 + i * 4) = xs.z; }
    } else LGKM0();
    SBAR();
    if (c >= 0) {
      av[0] = mfma32(xf[0][2 * c], wf[c & 1][1], av[0]);
      av[1] = mfma32(xf[1][2 * c], wf[c & 1][1], av[1]);
    }
    if (c < 7) {
      XRD(0, c + 1, 0); XRD(1, c + 1, 0); XRD(0, c + 1, 1); XRD(1, c + 1, 1);
      cf = trfrag(kC + (c + 1) * 4096, 256);
    } else {
#pragma unroll
      for (int o = 0; o < 2; ++o) { mr[o] = *(LAS f4*)(L + L_STT + (w ^ o) * 128 + g * 16); rs[o] = *(LAS f4*)(L + L_STT + (w ^ o) * 128 + 64 + g * 16); }
      c2k = *(LAS f2*)(L + L_CKV + (hd * 16 + i) * 8); c2v = *(LAS f2*)(L + L_CKV + (32 + hd * 16 + i) * 8);
    }
    if (c < 5) WLOAD(c + 3);
    if (c == 5) LD16(xs, dWS, i * 16, WS_XS + (p * 128 + w * 16) * 16);
    SB();
    if (c >= 0) {
      ak[0] = mfma32(xf[0][2 * c + 1], wf[c & 1][2], ak[0]);
      ak[1] = mfma32(xf[1][2 * c + 1], wf[c & 1][2], ak[1]);
    }
    if (c < 7) { wf[(c + 1) & 1][0] = WFRAG(2 * c + 2, 0); wf[(c + 1) & 1][1] = WFRAG(2 * c + 2, 1); }
    if (c < 5) XLOAD1(c + 3, 0); else if (c < 7) QLOAD1(2 * c - 10);
    SB();
    if (c >= 0) {
      av[0] = mfma32(xf[0][2 * c + 1], wf[c & 1][3], av[0]);
      av[1] = mfma32(xf[1][2 * c + 1], wf[c & 1][3], av[1]);
    }
    if (c < 7) { wf[(c + 1) & 1][2] = WFRAG(2 * c + 3, 0); wf[(c + 1) & 1][3] = WFRAG(2 * c + 3, 1); }
    if (c < 5) XLOAD1(c + 3, 1); else if (c < 7) QLOAD1(2 * c - 9);
    if (c == 7) {
      st16(pr1, rWS, lane * 16, w < 7 ? WS_P1 + ((j * 16 + p) * 7 + pos1) * 1024 : WS_BYTES);
      if (i == 0) st16(__builtin_bit_cast(v4u, prc), rWS, g * 16, w < 7 ? WS_PC + ((j * 7 + pos1) * 16 + p) * 64 : WS_BYTES);
      if (w >= 6) st16(pro, rWS, lane * 16, WS_PO + ((c4 * 16 + p) * 4 + 2 * th + (w & 1)) * 1024);
    }
    SB();
    if (c < 7) {
      cs = mfma32(ones, cf, cs);
      if (c + 1 == cres) { xres = ldf(L + ((c + 1) % 3 == 0 ? L_KVRED + tile2 * 2048 : L_WO + tile2 * 4096 + ((c + 1) % 3 - 1) * 2048) + (xrd ^ (hd * 64))); LGKM0(); }
    }
    if (c == -1) {
      rawload();
  tag = __builtin_amdgcn_readfirstlane(tag) + 1u;
  {
    float s = dot2(xrow.x, ONE2, dot2(xrow.y, ONE2, 0.f)) + dot2(xrow.z, ONE2, dot2(xrow.w, ONE2, 0.f));
    float ss = dot2(xrow.x, xrow.x, dot2(xrow.y, xrow.y, 0.f)) + dot2(xrow.z, xrow.z, dot2(xrow.w, xrow.w, 0.f));
    s = rowsum4(dppadd<0x128>(dppadd<0x124>(dppadd<0x122>(dppadd<0x121>(s))))) * (1.f / 512.f);
    ss = rowsum4(dppadd<0x128>(dppadd<0x124>(dppadd<0x122>(dppadd<0x121>(ss))))) * (1.f / 512.f);
    const float r = __builtin_amdgcn_rsqf(fmaxf(ss - s * s, 0.f) + 1e-5f);
    if (lane == 0) st16(v4u{asu(s), tag, asu(r), tag}, rWS, 0, WS_XS + (p * 128 + j * 8 + w) * 16);
  }
    }
    if (c == RAWAT + 3) {
      const int il = (pp * 2 + (w & 1)) * 8;
      pr1 = trfrag(L + L_RAW + (8 * g + q) * 224 + pp * 8 + w * 32, 4 * 224);
      pro = trfrag(L + L_RAW + 7168 + (8 * g + q) * 64 + il, 256);
      asm volatile("" :: "v"(tch));
    }
    if (c == RAWAT + 4) prc = mfma32(pr1, ones, z4);
    if (c == 6 && g == 0) *(LAS float*)(L + L_CKV + ((w & 3) * 16 + i) * 8 + (w >> 2) * 4) = cs[0];
  }
  STAMP(3);
  v4u w2r[28];
  const __amdgpu_buffer_rsrc_t rW2 = __builtin_amdgcn_make_buffer_rsrc((void*)w2, 0, 1792 * 512 * 2, 0x00020000);
  const int vW2 = (((lane >> 3) & 1) * 16 + g * 4) * 1024 + (lane & 7) * 16, vW2n = (((lane >> 3) & 1) * 4 + g * 8) * 1024 + (lane & 7) * 16, sW2 = 224 * w * 1024 + c4 * 128;
#define W2L(n) w2r[n] = __builtin_amdgcn_raw_buffer_load_b128(rW2, (((n) >> 2) == 3 ? vW2n : vW2) + ((n) & 3) * 1024, sW2 + ((n) >> 2) * 32768, 0)
  {
    const float ck = c2k[0] + c2k[1], cv = c2v[0] + c2v[1];
    float kk[8], vv[8];
#pragma unroll
    for (int n = 0; n < 8; ++n) {
      kk[n] = phi((ak[n >> 2][n & 3] - mr[n >> 2][n & 3] * ck) * rs[n >> 2][n & 3]);
      vv[n] = (av[n >> 2][n & 3] - mr[n >> 2][n & 3] * cv) * rs[n >> 2][n & 3];
    }
    const v4u pkf = {pk(kk[0], kk[1]), pk(kk[2], kk[3]), pk(kk[4], kk[5]), pk(kk[6], kk[7])};
    const v4u pvf = {pk(vv[0], vv[1]), pk(vv[2], vv[3]), pk(vv[4], vv[5]), pk(vv[6], vv[7])};
    const f4 kv = mfma32(pkf, pvf, z4), z = mfma32(pkf, ones, z4);
    *(LAS f4*)(Ll + L_KVRED + w * 1024) = kv;
    if (i == 0) *(LAS f4*)(L + L_ZP + w * 64 + g * 16) = z;
  }
  if (w < 6) VMWAIT(2); else VMWAIT(3);
  BAR();
  STAMP(4);
  if (w < 2) {
    __builtin_amdgcn_s_setprio(3);
    f4 t[8];
#pragma unroll
    for (int n = 0; n < 4; ++n) { t[n] = *(LAS f4*)(Ll + L_KVRED + (n * 2 + w) * 1024); t[4 + n] = *(LAS f4*)(L + L_ZP + (n * 2 + w) * 64 + pp * 16); }
    SB();
    const int so = WS_KV + ((j * 2 + w) * 16 + p) * KV_REC;
    const f4 a = (t[0] + t[1]) + (t[2] + t[3]), z = (t[4] + t[5]) + (t[6] + t[7]);
    st16(v4u{pk(a[0], a[1]), tag, pk(a[2], a[3]), tag}, rWS, lane * 16, so);
    if (lane < 8) st16(v4u{pk(z[0], z[1]), tag, pk(z[2], z[3]), tag}, rWS, lane * 16 + 1024, so);
    STAMP(2);
  }
  v2u pq[2];
  v4u kt[5];
  const unsigned kso = WS_KV + ((j * 2 + hd) * 16 + 4 * (w >> 1)) * KV_REC;
  {
    f4 aq[2] = {z4, z4}, cq = z4;
    v4u qf[4];
    float m1[2], r1[2];
    f4 cp[4];
#pragma unroll
    for (int s = 0; s < 3; ++s) qf[s] = trfrag(tN + L_WQ + s * 2048, 256);
    {
      v4u t[4];
#pragma unroll
      for (int n = 0; n < 4; ++n) t[n] = trfrag(tN + L_WQ + (w >> 1) * 8192 + n * 2048, 256);
#pragma unroll
      for (int n = 0; n < 4; ++n) cq = mfma32(t[n], ones, cq);
    }
#pragma unroll
    for (int s = 0; s < 16; ++s) {
      if (s + 3 < 16) qf[(s + 3) & 3] = trfrag(tN + L_WQ + (s + 3) * 2048, 256);
      aq[0] = mfma32(qf[s & 3], xf[0][s], aq[0]);
      aq[1] = mfma32(qf[s & 3], xf[1][s], aq[1]);
      if (s == 1 && i == 0) *(LAS f4*)(L + L_CQ + w * 64 + g * 16) = cq;
      if (s >= 2 && s < 2 + W2Q) W2L(s - 2);
      if (s == 13) {
#pragma unroll
        for (int o = 0; o < 2; ++o) { m1[o] = *(LAS float*)(L + L_STT + (w ^ o) * 128 + i * 4); r1[o] = *(LAS float*)(L + L_STT + (w ^ o) * 128 + 64 + i * 4); }
      }
      if (s == 14) {
        VMWAIT((KVAT < 14 ? 5 : 0) + ((XP & 1) ? 0 : W2Q));
        __builtin_amdgcn_s_setprio(0);
        SBAR();
#pragma unroll
        for (int n = 0; n < 4; ++n) cp[n] = *(LAS f4*)(L + L_CQ + (2 * n + hd) * 64 + g * 16);
      }
      if (s == KVAT) {
#pragma unroll
        for (int n = 0; n < 4; ++n) LD16(kt[n], dWS, lane * 16, kso + n * KV_REC);
        LD16(kt[4], dWS, q * KV_REC + pp * 16 + 1024, kso);
      }
      SB();
    }
#pragma unroll
    for (int n = W2Q; n < W2Q + W2R; ++n) W2L(n);
    SB();
    if (tid == 128) __hip_atomic_store(myflag, tag, RLX_AGENT);
    cq = (cp[0] + cp[1]) + (cp[2] + cp[3]);
#pragma unroll
    for (int o = 0; o < 2; ++o) {
      const float m = m1[o], r = r1[o];
      float a[4];
#pragma unroll
      for (int n = 0; n < 4; ++n) a[n] = phi((aq[o][n] - m * cq[n]) * r);
      pq[o] = v2u{pk(a[0], a[1]), pk(a[2], a[3])};
    }
    asm volatile("" : "+v"(pq[0]), "+v"(pq[1]));
  }
  STAMP(5);
  {
    if (KVAT == 16) {
#pragma unroll
      for (int n = 0; n < 4; ++n) LD16(kt[n], dWS, lane * 16, kso + n * KV_REC);
      LD16(kt[4], dWS, q * KV_REC + pp * 16 + 1024, kso);
      VMWAIT(0);
    }
    asm volatile("s_waitcnt vmcnt(%5)" : "+v"(kt[0]), "+v"(kt[1]), "+v"(kt[2]), "+v"(kt[3]), "+v"(kt[4]) : "i"((XP & 1) ? 0 : W2R) : "memory");
#pragma nounroll
    for (unsigned sp = 0; sp < SPIN; ++sp) {
      unsigned bad = 0;
#pragma unroll
      for (int n = 0; n < 5; ++n) bad |= (kt[n].y ^ tag) | (kt[n].w ^ tag);
      if (none(bad != 0)) break;
      COUNT(24);
#pragma unroll
      for (int n = 0; n < 4; ++n) LD16(kt[n], dWS, lane * 16, kso + n * KV_REC);
      LD16(kt[4], dWS, q * KV_REC + pp * 16 + 1024, kso);
      asm volatile("s_waitcnt vmcnt(0)" : "+v"(kt[0]), "+v"(kt[1]), "+v"(kt[2]), "+v"(kt[3]), "+v"(kt[4]) :: "memory");
    }
    STAMP(6);
    const f4 s = {(bflo(kt[0].x) + bflo(kt[1].x)) + (bflo(kt[2].x) + bflo(kt[3].x)), (bfhi(kt[0].x) + bfhi(kt[1].x)) + (bfhi(kt[2].x) + bfhi(kt[3].x)),
                  (bflo(kt[0].z) + bflo(kt[1].z)) + (bflo(kt[2].z) + bflo(kt[3].z)), (bfhi(kt[0].z) + bfhi(kt[1].z)) + (bfhi(kt[2].z) + bfhi(kt[3].z))};
    *(LAS f4*)(Ll + L_CONS + w * 1024) = s;
    const f4 z = {dppadd<0x128>(dppadd<0x124>(bflo(kt[4].x))), dppadd<0x128>(dppadd<0x124>(bfhi(kt[4].x))),
                  dppadd<0x128>(dppadd<0x124>(bflo(kt[4].z))), dppadd<0x128>(dppadd<0x124>(bfhi(kt[4].z)))};
    if (lane < 4) *(LAS f4*)(L + L_ZC + w * 64 + lane * 16) = z;
  }
#pragma unroll
  for (int n = W2Q + W2R; n < W2Q + W2R + W2S; ++n) W2L(n);
  BAR();
  STAMP(7);
  unsigned pf = tag;
  if (w == 0) asm volatile("buffer_load_dword %0, %1, %2, %3 offen sc1" : "+v"(pf) : "v"((i * 16 + (j ^ ((g & 1) << 3))) * 128), "s"(dWS), "s"(WS_FLAG) : "memory");
  const int ohoff = ((p * 8 + w) * 16 + j) * 1024;
  {
    f4 kq[4], zq[4];
#pragma unroll
    for (int n = 0; n < 4; ++n) { kq[n] = *(LAS f4*)(Ll + L_CONS + (2 * n + hd) * 1024); zq[n] = *(LAS f4*)(L + L_ZC + (2 * n + hd) * 64 + g * 16); }
    SB();
    const f4 k0 = (kq[0] + kq[1]) + (kq[2] + kq[3]), z0 = (zq[0] + zq[1]) + (zq[2] + zq[3]);
    const v2u ka = {pk(k0[0], k0[1]), pk(k0[2], k0[3])}, za = {pk(z0[0], z0[1]), pk(z0[2], z0[3])};
    const f4 n0 = mfma16(ka, pq[0], z4), n1 = mfma16(ka, pq[1], z4), d0 = mfma16(za, pq[0], z4), d1 = mfma16(za, pq[1], z4);
    const float r0 = __builtin_amdgcn_rcpf(d0[0] + 1e-6f), r1 = __builtin_amdgcn_rcpf(d1[0] + 1e-6f);
    const auto sx = __builtin_amdgcn_permlane16_swap(pk(n0[0] * r0, n0[1] * r0), pk(n1[0] * r1, n1[1] * r1), false, false);
    const auto sy = __builtin_amdgcn_permlane16_swap(pk(n0[2] * r0, n0[3] * r0), pk(n1[2] * r1, n1[3] * r1), false, false);
    st16(v4u{sx[0], sy[0], sx[1], sy[1]}, rWS, ((hd ^ g) & 1) * 16384 + ((2 * hd + (g >> 1)) * 16 + i) * 16, WS_O + ohoff - hd * 16384);
  }
  constexpr int W2E = W2Q + W2R + W2S + W2T;
#pragma unroll
  for (int n = W2E - W2T; n < W2E; ++n) W2L(n);
  asm volatile("s_waitcnt vmcnt(%1)" : "+v"(pf) : "i"((XP & 1) ? 0 : W2T) : "memory");
  if (!none(pf != tag)) poll(flags + (i * 16 + (j ^ ((g & 1) << 3))) * 32, tag, 32, lane);
  SBAR();
  if (tid == 0) __hip_atomic_store(myflag + 256 * 32, tag, RLX_AGENT);
  STAMP(20);
  const int r4 = RT3 ? (j & 1) * 4 : 0;
  {
    const unsigned so = WS_PO + (c4 * 16 + ((2 * w) & 8) + ((2 * w + r4) & 7)) * 4096;
    if (!(XP & 2)) { dmac<4>(dWS, lane * 16, so, lds0 + L_WO2 + w * 8192); dmac<4>(dWS, lane * 16, so + 4096, lds0 + L_WO2 + w * 8192 + 4096); }
  }
  v4u ct;
  unsigned bias;
  LD16(ct, dWS, lane * 16, WS_PC + (j * 7 + w - 1) * 1024);
  asm volatile("buffer_load_ushort %0, %1, %2, 0 offen" : "=v"(bias) : "v"((((w + 6 + (j & 1)) % 7) * 16 + pp * 4 + q) * 2), "s"(mksrd(b1 + 112 * j, 224)) : "memory");
  w2pack(w2r); SB();
  w2pack(w2r + 4); SB();
  if (w == 0) poll(flags + (256 + p * 16 + i) * 32, tag, 16, lane);
  asm volatile("s_waitcnt vmcnt(0)" : "+v"(ct), "+v"(bias) :: "memory");
  SBAR();
  STAMP(8);
#define LOAD1(s) f[s] = __builtin_amdgcn_raw_buffer_load_b128(rWS, lane * 16 + ((s) & 3) * 1024, so + ((((s) & 12) + rot4) & 15) * 1024, SC1)
#define LOADPD() do { _Pragma("unroll") for (int s = 0; s < PD; ++s) { LOAD1(s); SB(); } } while (0)
  v4u hres;
  {
    const unsigned so = WS_O + (p * 8 + tile2) * 16384 + hd * 8192;
    v4u f[8];
    LAS char* const Lo = opq(Ll + L_WO2 + hd * (32768 + 2048));
    LAS char* const Lp = opq(Ll + L_WO2 + hd * (32768 - 2048) + 2048);
    SFOR(s, 0, 8, 1)
      ld16i<(s & 3) * 1024>(f[s], dWS, lane * 16, so + (((s & 4) + r4) & 7) * 1024);
    SEND
    v4u wf2[2][4];
#pragma unroll
    for (int t = 0; t < 4; ++t) wf2[0][t] = ldf((t < 2 ? Lo : Lp) + (t & 1) * 1024);
    f4 a[4] = {f4{bflo(xres.x), bfhi(xres.x), bflo(xres.y), bfhi(xres.y)}, f4{bflo(xres.z), bfhi(xres.z), bflo(xres.w), bfhi(xres.w)}, z4, z4};
    SFOR(s, 0, 8, 1)
      if (s + 1 < 8) {
#pragma unroll
        for (int t = 0; t < 4; ++t) wf2[(s + 1) & 1][t] = ldf((t < 2 ? Lo : Lp) + (s + 1) * 4096 + (t & 1) * 1024);
      }
      vmpin<7 - s + ((XP & 2) ? 0 : (s < P2T ? s : P2T))>(f[s]);
      SB();
#pragma unroll
      for (int t = 3; t >= 0; --t) a[t] = mfma32(wf2[s & 1][t], f[s], a[t]);
      if (s < P2T) P1L(1, w, s);
      SB();
    SEND
    *(LAS f4*)(Ll + L_EX + w * 2048) = a[2];
    *(LAS f4*)(Ll + L_EX + w * 2048 + 1024) = a[3];
    BAR();
    const f4 a0 = a[0] + *(LAS f4*)(Ll + L_EX + (w ^ 1) * 2048), a1 = a[1] + *(LAS f4*)(Ll + L_EX + (w ^ 1) * 2048 + 1024);
    hres = v4u{pk(a0[0], a0[1]), pk(a0[2], a0[3]), pk(a1[0], a1[1]), pk(a1[2], a1[3])};
    const int ho = ((p * 8 + tile2) * 16 + 2 * c4 + hd) * 1024;
    st16(hres, rWS, lane * 16, WS_H + ho);
    const f4 hs = mfma32(ones, hres, z4), hg = mfma32(hres, hres, z4);
    if (g == q) __builtin_amdgcn_raw_buffer_store_b64(v2u{asu(hs[0]), asu(pp == 0 ? hg[0] : pp == 1 ? hg[1] : pp == 2 ? hg[2] : hg[3])}, rWS, i * 8, WS_S + (ho >> 3), SC1);
    if (bid == 0 && tid == 0) __builtin_amdgcn_raw_buffer_store_b32(tag, rWS, WS_EPOCH, 0, SC1);
  }
  STAMP(9);
  SFOR(n, P2T, 7, 1) P1L(1, w, n); SEND
    if (w) {
      const f4 c = {rowsum4(dppadd<0x128>(dppadd<0x124>(asf(ct.x)))), rowsum4(dppadd<0x128>(dppadd<0x124>(asf(ct.y)))),
                    rowsum4(dppadd<0x128>(dppadd<0x124>(asf(ct.z)))), rowsum4(dppadd<0x128>(dppadd<0x124>(asf(ct.w))))};
      const float cf = q == 0 ? c[0] : q == 1 ? c[1] : q == 2 ? c[2] : c[3];
      const unsigned ch = pk(cf, cf), cl = pk(cf - bflo(ch), 0.f);
      if (g == 0) *(LAS v4u*)(L + L_C1 + ((w - 1) * 16 + pp * 4 + q) * 16) = v4u{ch, (cl & 0xffffu) | (bias << 16), bias, 0u};
    }
  VMWAIT((XP & 2) ? 0 : 7 - P2T);
  BAR();
  if (tid == 0) __hip_atomic_store(myflag + 512 * 32, tag, RLX_AGENT);
  STAMP(21);
  P1L(4, 8 + w, 0); P1L(3, 8 + w, 4);
#pragma unroll
  for (int s = 2; s < W2E / 4; ++s) { w2pack(w2r + 4 * s); SB(); }
  v4u cb[7];
#pragma unroll
  for (int t = 0; t < 7; ++t) cb[t] = ldf(L + L_C1 + (t * 16 + i) * 16);
  if (w == 0) poll(flags + (512 + p * 16 + i) * 32, tag, 16, lane);
  VMWAIT(0);
  SBAR();
  STAMP(10);
  constexpr int W2B = W2E + W2P;
  {
    v4u f[16];
    const int so = WS_H + (p * 8 + w) * 16384;
    LAS char* const wp0 = opq(Ll + L_W1);
    LAS char* const wp1 = opq(wp0 + 8 * 7168);
#define TS(n) ((n) < 16 * NA ? (n) / NA : ((n) - 16 * NA) / (NA < 7 ? 7 - NA : 1))
#define TT(n) ((n) < 16 * NA ? (n) % NA : NA + ((n) - 16 * NA) % (NA < 7 ? 7 - NA : 1))
#define W1F(n) ldf((TS(n) < 8 ? wp0 : wp1) + (TS(n) & 7) * 7168 + TT(n) * 1024)
    v2u e[7];
    const int aoff = WS_A + ((p * 8 + w) * 56 + ((7 * j + 1) >> 1)) * 1024;
    f4 acc[7];
    if (PRIO3 == 1 || (PRIO3 == 2 && w >= 4)) __builtin_amdgcn_s_setprio(2);
    v2u st[4];
#pragma unroll
    for (int n = 0; n < 4; ++n) st[n] = __builtin_amdgcn_raw_buffer_load_b64(rWS, g * 512 + i * 8 + n * 128, WS_S + (p * 8 + w) * 2048, SC1);
    SB();
    LOADPD();
    float rstd2;
    {
      v4u wf3[6];
#pragma unroll
      for (int n = 0; n < 4; ++n) wf3[n] = W1F(n);
      SB();
      {
        const float mean2 = rowsum4((asf(st[0].x) + asf(st[1].x)) + (asf(st[2].x) + asf(st[3].x))) * (1.f / 512.f);
        const float var = fmaxf(rowsum4((asf(st[0].y) + asf(st[1].y)) + (asf(st[2].y) + asf(st[3].y))) * (1.f / 512.f) - mean2 * mean2, 0.f) + 1e-5f;
        rstd2 = __builtin_amdgcn_rsqf(var);
        const float sig = var * rstd2;
        const unsigned msk = g ? 0u : ~0u;
        const unsigned hi = pk(-mean2, sig), lo = pk(-mean2 - bflo(hi), sig - bfhi(hi));
        const v4u ex = {((hi & 0xffffu) | (lo << 16)) & msk, hi & msk, (lo >> 16) & msk, 0u};
#pragma unroll
        for (int t = 0; t < 7; ++t) acc[t] = mfma32(cb[t], ex, z4);
      }
      auto epi = [&](int t0, int t1) __attribute__((always_inline)) TGT {
        if (PRIO3) __builtin_amdgcn_s_setprio(0);
#pragma unroll
        for (int t = t0; t < t1; ++t) {
          const f4 a = acc[t] * (rstd2 * LOG2E);
          const f4 m = f4{__builtin_amdgcn_exp2f(a[0]), __builtin_amdgcn_exp2f(a[1]), __builtin_amdgcn_exp2f(a[2]), __builtin_amdgcn_exp2f(a[3])} + 1.f;
          const f4 d = m * m + 1.f;
          const f4 y = acc[t] * (f4{__builtin_amdgcn_rcpf(d[0]), __builtin_amdgcn_rcpf(d[1]), __builtin_amdgcn_rcpf(d[2]), __builtin_amdgcn_rcpf(d[3])} * (-2.f * rstd2) + rstd2);
          e[t] = v2u{pk(y[0], y[1]), pk(y[2], y[3])};
          if (t & 1) { st16(v4u{e[t - 1].x, e[t - 1].y, e[t].x, e[t].y}, rWS, lane * 16 + (t >> 1) * 1024, aoff); SB(); }
        }
        SB();
      };
      SFOR(n, 0, 112, 2)
        if (n == 16 * NA) { STAMP(12); epi(0, NA); if (PRIO3) __builtin_amdgcn_s_setprio(2); STAMP(19); }
        if (n + 4 < 112) { wf3[(n + 4) % 6] = W1F(n + 4); wf3[(n + 5) % 6] = W1F(n + 5); }
        SB();
        acc[TT(n + 1)] = mfma32(wf3[(n + 1) % 6], f[TS(n + 1)], acc[TT(n + 1)]);
        acc[TT(n)] = mfma32(wf3[n % 6], f[TS(n)], acc[TT(n)]);
        constexpr int k = n / 4 + PD;
        if ((n & 2) == 0) {
          if (k < 16) LOAD1(k < 16 ? k : 0);
          else if (W2E + k - 16 < W2B) W2L(k > 15 && W2E + k - 16 < W2B ? W2E + k - 16 : 0);
        }
        SB();
      SEND
      STAMP(11);
      epi(NA < 7 ? NA : 0, 7);
    }
#define SINGLE(v, off) do { const auto a_ = __builtin_amdgcn_permlane16_swap(v.x, v.x, false, false), b_ = __builtin_amdgcn_permlane16_swap(v.y, v.y, false, false); \
      if ((g & 1) == 0) st16(v4u{a_[0], b_[0], a_[1], b_[1]}, rWS, (g >> 1) * 256 + i * 16, aoff + (off)); } while (0)
    SINGLE(e[6], (j & 1) ? -512 : 3072);
  }
  STAMP(13);
#pragma unroll
  for (int n = W2B; n < W2B + W2X; ++n) W2L(n);
  VMWAIT((XP & 1) ? 0 : W2X);
  BAR();
  if (tid == 0) __hip_atomic_store(myflag + 768 * 32, tag, RLX_AGENT);
  STAMP(14);
#pragma unroll
  for (int n = W2B + W2X; n < 28; ++n) W2L(n);
  SB();
  *(LAS v4u*)(L + L_HRES + w * 1024 + (16 * g + (i ^ (4 * g))) * 16) = hres;
#pragma unroll
  for (int s = W2E / 4; s < (W2B + W2X) / 4; ++s) { w2pack(w2r + 4 * s); SB(); }
  poll(flags + (768 + p * 16 + 2 * w + (lane & 1)) * 32, tag, 2, lane);
  STAMP(15);
  {
    const int so = WS_A + ((p * 8 + 4 * th) * 56 + 7 * w) * 1024;
    constexpr int NB = NB4;
    if (PRIO4 && w >= 4) __builtin_amdgcn_s_setprio(1);
    const int rt = RT4 ? j & 3 : 0;
    v4u f[NB];
#define ALOAD(n) __builtin_amdgcn_raw_buffer_load_b128(rWS, lane * 16 + (((n) % 7) & 3) * 1024, so + (((((n) / 7) + rt) & 3) * 56 + (((n) % 7) & 4)) * 1024, SC1)
#pragma unroll
    for (int n = 0; n < NB; ++n) { f[n] = ALOAD(n); SB(); }
#pragma unroll
    for (int s = (W2B + W2X) / 4; s < 7; ++s) { w2pack(w2r + 4 * s); SB(); }
    const int wr = (w * 1024 + 16 * g + (i ^ ((g & 1) << 3))) * 16;
    f4 acc[2][4];
#define PUT(t, r) *(LAS f4*)(L + (wr ^ ((r) << 5)) + (((t) + rt) & 3) * 4096 + (r) * 1024) = f4{acc[(t) & 1][0][r], acc[(t) & 1][1][r], acc[(t) & 1][2][r], acc[(t) & 1][3][r]}
#pragma unroll
    for (int n = 0; n < 28; ++n) {
      const int s = n % 7, t = n / 7;
#pragma unroll
      for (int u = 0; u < 4; ++u) acc[t & 1][u] = mfma32(w2r[s * 4 + u], f[n % NB], s ? acc[t & 1][u] : z4);
      if (n + NB < 28) f[n % NB] = ALOAD(n + NB < 28 ? n + NB : 0);
      if (DEFER && t && s >= 1 && s < 5) PUT(t - 1, (s - 1) & 3);
      if (s == 6 && (!DEFER || t == 3)) {
#pragma unroll
        for (int r = 0; r < 4; ++r) PUT(t, r);
      }
      SB();
    }
    STAMP(16);
    BAR();
    STAMP(17);
    const int tk = hd * 8 + (lane >> 3);
    LAS char* const rp = opq(L + (w >> 1) * 4096 + pp * 1024 + (16 * (q & 1) + (tk ^ ((q & 1) << 3) ^ (pp << 1))) * 16);
    LAS char* const rq = opq(rp + 65536);
    hres = *(LAS v4u*)(L + L_HRES + ((w & 6) + (q & 1)) * 1024 + (16 * pp + (tk ^ (4 * pp))) * 16);
    f4 t[16];
#pragma unroll
    for (int n = 0; n < 16; ++n) t[n] = *(LAS f4*)((n & 4 ? rq : rp) + (n & 3) * 16384 + (n >> 3) * 512);
    SB();
    const f4 a0 = ((t[0] + t[1]) + (t[2] + t[3])) + ((t[4] + t[5]) + (t[6] + t[7]));
    const f4 a1 = ((t[8] + t[9]) + (t[10] + t[11])) + ((t[12] + t[13]) + (t[14] + t[15]));
    const v4u o = {pk(a0[0] + bflo(hres.x), a0[1] + bfhi(hres.x)), pk(a0[2] + bflo(hres.y), a0[3] + bfhi(hres.y)),
                   pk(a1[0] + bflo(hres.z), a1[1] + bfhi(hres.z)), pk(a1[2] + bflo(hres.w), a1[3] + bfhi(hres.w))};
    __builtin_amdgcn_raw_buffer_store_b128(o, __builtin_amdgcn_make_buffer_rsrc(out, 0, 2048 * 512 * 2, 0x00020000), ((p * 128 + th * 64 + w * 8 + (lane >> 3)) * 512 + 64 * c4 + 8 * (lane & 7)) * 2, 0, OUTAUX);
  }
  STAMP(18);
  STAMP_FLUSH
}

extern "C" void kernel_launch(void* const* d_in, const int* in_sizes, int n_in, void* d_out, int out_size, void* d_ws, size_t ws_size,
                              hipStream_t stream) {
  (void)in_sizes; (void)n_in; (void)out_size; (void)ws_size;
  (void)hipFuncSetAttribute((const void*)fused_block, hipFuncAttributeMaxDynamicSharedMemorySize, LDS_BYTES);
  fused_block<<<256, 512, LDS_BYTES, stream>>>((const u16*)d_in[0], (const u16*)d_in[1], (const u16*)d_in[2], (const u16*)d_in[4],
                                               (const u16*)d_in[5], (const u16*)d_in[6], (u16*)d_out, (char*)d_ws);
}
```

```cpp
#include <hip/hip_runtime.h>
#include <hip/hip_bf16.h>

typedef unsigned short u16;
typedef unsigned v4u __attribute__((ext_vector_type(4)));
typedef unsigned v2u __attribute__((ext_vector_type(2)));
typedef float f4 __attribute__((ext_vector_type(4)));
typedef float f2 __attribute__((ext_vector_type(2)));
typedef short s4 __attribute__((ext_vector_type(4)));
typedef __bf16 b8 __attribute__((ext_vector_type(8)));
typedef __bf16 b2 __attribute__((ext_vector_type(2)));
#define GAS __attribute__((address_space(1)))
#define LAS __attribute__((address_space(3)))
#if defined(__HIP_DEVICE_COMPILE__)
#define TGT __attribute__((target("no-packed-fp32-ops")))
#else
#define TGT
#endif
#define DI __device__ __forceinline__ TGT
#define RLX_AGENT __ATOMIC_RELAXED, __HIP_MEMORY_SCOPE_AGENT

constexpr int CODE_KB = 22;
constexpr int PD = 6, NB = 5;
constexpr int W2Q = 4, W2R = 4, W2S = 4, W2T = 4, W2P = 8, W2X = 4;
constexpr int P2T = 4;
constexpr int NA = 4;
constexpr int WS_EPOCH = 0;
constexpr int WS_FLAG = 4096;
constexpr int WS_XS = 196608;
constexpr int WS_KV = 262144;
constexpr int KV_REC = 1152;
constexpr int WS_O = 1 << 20;
constexpr int WS_H = 3 << 20;
constexpr int WS_A = 5 << 20;
constexpr int WS_S = 12 << 20;
constexpr int WS_P1 = 13 << 20;
constexpr int WS_PO = 15 << 20;
constexpr int WS_PC = WS_PO + (1 << 19);
constexpr int WS_BYTES = 16 << 20;

constexpr int L_WQ = 0, L_WK = 32768, L_KVRED = 98304, L_WO = 114688, L_SCR = 147456;
constexpr int L_RAW = 0;
constexpr int L_W1 = 0;
constexpr int L_CONS = L_SCR;
constexpr int L_ZP = L_SCR + 8192;
constexpr int L_ZC = L_ZP + 512;
constexpr int L_CKV = L_ZC + 512;
constexpr int L_STT = L_CKV + 512;
constexpr int L_C1 = L_STT + 1024;
constexpr int L_CQ = L_C1 + 2048;
constexpr int L_WO2 = 81920, L_EX = 57344;
constexpr int L_HRES = 131072;
constexpr int LDS_BYTES = 163840;

constexpr unsigned ONE2 = 0x3F803F80u;
constexpr int SC1 = 16;
constexpr float LOG2E = 1.4426950408889634f;
constexpr unsigned SPIN = 1u << 15;

DI float asf(unsigned u) { return __builtin_bit_cast(float, u); }
DI unsigned asu(float f) { return __builtin_bit_cast(unsigned, f); }
DI f4 mfma32(v4u a, v4u b, f4 c) { return __builtin_amdgcn_mfma_f32_16x16x32_bf16(__builtin_bit_cast(b8, a), __builtin_bit_cast(b8, b), c, 0, 0, 0); }
DI f4 mfma16(v2u a, v2u b, f4 c) { return __builtin_amdgcn_mfma_f32_16x16x16bf16_1k(__builtin_bit_cast(s4, a), __builtin_bit_cast(s4, b), c, 0, 0, 0); }
DI unsigned pk(float lo, float hi) { f2 v = {lo, hi}; return __builtin_bit_cast(unsigned, __builtin_convertvector(v, b2)); }
DI float bflo(unsigned u) { return asf(u << 16); }
DI float bfhi(unsigned u) { return asf(u & 0xffff0000u); }
DI float rb(float v) { return bflo(pk(v, v)); }
DI float ln_sigma(float var) { return __builtin_amdgcn_sqrtf(rb(rb(fmaxf(var, 0.f)) + 1e-5f)); }
DI v2u trd(LAS char* p) { return __builtin_bit_cast(v2u, __builtin_amdgcn_ds_read_tr16_b64_v4i16((LAS s4*)p)); }
DI v4u trfrag(LAS char* p, int second) { v2u a = trd(p), b = trd(p + second); return v4u{a.x, a.y, b.x, b.y}; }
DI v4u ldf(LAS char* p) { return *(LAS v4u*)p; }
DI float dot2(unsigned a, unsigned b, float c) { return __builtin_amdgcn_fdot2_f32_bf16(__builtin_bit_cast(b2, a), __builtin_bit_cast(b2, b), c, false); }
template <int C> DI float dppadd(float v) { return v + asf(__builtin_amdgcn_mov_dpp(asu(v), C, 0xf, 0xf, true)); }
DI float rowsum4(float v) {
  auto r = __builtin_amdgcn_permlane32_swap(asu(v), asu(v), false, false);
  const float s = asf(r[0]) + asf(r[1]);
  auto t = __builtin_amdgcn_permlane16_swap(asu(s), asu(s), false, false);
  return asf(t[0]) + asf(t[1]);
}
DI bool none(bool c) { return __builtin_amdgcn_ballot_w64(c) == 0; }
DI v4u mksrd(const void* p, unsigned bytes) {
  unsigned long long a = (unsigned long long)p;
  return v4u{(unsigned)a, (unsigned)(a >> 32) & 0xffffu, bytes, 0x00020000u};
}
DI void dma16(v4u srd, unsigned voff, unsigned soff, unsigned lds) {
  asm volatile("s_mov_b32 m0, %3\n\ts_nop 0\n\tbuffer_load_dwordx4 %0, %1, %2 offen lds" ::"v"(voff), "s"(srd), "s"(soff), "s"(lds) : "memory");
}
template <int N> DI void dmac(v4u srd, unsigned voff, unsigned so, unsigned lds) {
  if (N == 1) asm volatile("s_mov_b32 m0, %3\n\ts_nop 0\n\tbuffer_load_dwordx4 %0, %1, %2 offen sc1 lds" ::"v"(voff), "s"(srd), "s"(so), "s"(lds) : "memory");
  if (N == 3) asm volatile("s_mov_b32 m0, %3\n\ts_nop 0\n\tbuffer_load_dwordx4 %0, %1, %2 offen sc1 lds\n\tbuffer_load_dwordx4 %0, %1, %2 offen offset:1024 sc1 lds\n\t"
                           "buffer_load_dwordx4 %0, %1, %2 offen offset:2048 sc1 lds" ::"v"(voff), "s"(srd), "s"(so), "s"(lds) : "memory");
  if (N == 4) asm volatile("s_mov_b32 m0, %3\n\ts_nop 0\n\tbuffer_load_dwordx4 %0, %1, %2 offen sc1 lds\n\tbuffer_load_dwordx4 %0, %1, %2 offen offset:1024 sc1 lds\n\t"
                           "buffer_load_dwordx4 %0, %1, %2 offen offset:2048 sc1 lds\n\tbuffer_load_dwordx4 %0, %1, %2 offen offset:3072 sc1 lds"
                           ::"v"(voff), "s"(srd), "s"(so), "s"(lds) : "memory");
}
#define LD16(dst, srd, voff, soff) asm volatile("buffer_load_dwordx4 %0, %1, %2, %3 offen sc1" : "=v"(dst) : "v"(voff), "s"(srd), "s"(soff) : "memory")
template <int OFF> DI void ld16i(v4u& d, v4u srd, unsigned voff, unsigned soff) {
  asm volatile("buffer_load_dwordx4 %0, %1, %2, %3 offen offset:%4 sc1" : "=v"(d) : "v"(voff), "s"(srd), "s"(soff), "i"(OFF) : "memory");
}
template <int N> DI void vmpin(v4u& d) { asm volatile("s_waitcnt vmcnt(%1)" : "+v"(d) : "i"(N)); }
DI void st16(v4u v, __amdgpu_buffer_rsrc_t r, int voff, int soff) {
  __builtin_amdgcn_raw_buffer_store_b128(v, r, voff, soff, SC1);
  asm volatile("s_nop 1" ::"v"(v) : "memory");
}
DI LAS char* opq(LAS char* p) { asm volatile("" : "+v"(p)); return p; }
#define LGKM0() asm volatile("s_waitcnt lgkmcnt(0)" ::: "memory")
#define SBAR() do { __builtin_amdgcn_s_barrier(); asm volatile("" ::: "memory"); } while (0)
#define BAR() do { LGKM0(); SBAR(); } while (0)
#define VMWAIT(n) asm volatile("s_waitcnt vmcnt(%0)" ::"i"(n) : "memory")
#define SB() __builtin_amdgcn_sched_barrier(0)

template <int V> struct IC { static constexpr int value = V; };
template <int B, int E, int S, class F> DI void sfor(F&& f) { if constexpr (B < E) { f(IC<B>{}); sfor<B + S, E, S>(f); } }
#define SFOR(n, B, E, S) sfor<B, E, S>([&](auto n##_) __attribute__((always_inline)) TGT { constexpr int n = decltype(n##_)::value;
#define SEND });
DI float phi(float t) { return fmaxf(t, 0.f) + __builtin_amdgcn_exp2f(fminf(t, 0.f) * LOG2E) + 1e-6f; }
DI void poll(GAS unsigned* fp, unsigned tag, int n, int lane) {
#pragma nounroll
  for (unsigned sp = 0; sp < SPIN; ++sp) {
    const unsigned v = lane < n ? __hip_atomic_load(fp, RLX_AGENT) : tag;
    if (none(v != tag)) break;
  }
  asm volatile("" ::: "memory");
}

DI void w2pack(v4u* r) {
  v2u lo[4], hi[4];
#pragma unroll
  for (int n = 0; n < 4; ++n)
#pragma unroll
    for (int d = 0; d < 2; ++d) {
      const unsigned a = r[n][d], b = r[n][2 + d];
      lo[n][d] = __builtin_amdgcn_update_dpp(a, b, 0x128, 0xf, 0xc, false);
      hi[n][d] = __builtin_amdgcn_update_dpp(b, a, 0x128, 0xf, 0x3, false);
    }
#pragma unroll
  for (int u = 0; u < 4; ++u) {
    const unsigned sel = (u & 1) ? 0x07060302u : 0x05040100u;
    r[u] = v4u{__builtin_amdgcn_perm(lo[1][u >> 1], lo[0][u >> 1], sel), __builtin_amdgcn_perm(lo[3][u >> 1], lo[2][u >> 1], sel),
               __builtin_amdgcn_perm(hi[1][u >> 1], hi[0][u >> 1], sel), __builtin_amdgcn_perm(hi[3][u >> 1], hi[2][u >> 1], sel)};
  }
  asm volatile("" : "+v"(r[0]), "+v"(r[1]), "+v"(r[2]), "+v"(r[3]));
}

__global__ void __launch_bounds__(512, 2) TGT
fused_block(const u16* x_, const u16* wqkv_, const u16* wo_, const u16* w1_, const u16* b1_, const u16* w2_, u16* out_, char* ws_) {
  extern __shared__ __attribute__((aligned(16))) char smem_[];
  typedef unsigned long long u64x8 __attribute__((ext_vector_type(8)));
  u64x8 ka;
  asm volatile("s_load_dwordx16 %0, %1, 0x0" : "=s"(ka) : "s"(__builtin_amdgcn_kernarg_segment_ptr()) : "memory");
  unsigned tch = 0;
  if ((blockIdx.x >> 3) < CODE_KB && threadIdx.x < 8) {
    unsigned long long pc;
    asm volatile("s_getpc_b64 %0" : "=s"(pc));
    asm volatile("global_load_dword %0, %1, %2" : "+v"(tch) : "v"(threadIdx.x * 128), "s"((pc & ~127ull) + (blockIdx.x >> 3) * 1024) : "memory");
  }
  LAS char* L = (LAS char*)smem_;
  const int tid = threadIdx.x, lane = tid & 63, w = __builtin_amdgcn_readfirstlane(tid >> 6);
  const int g = lane >> 4, i = lane & 15, q = (lane >> 2) & 3, pp = lane & 3;
  const int bid = blockIdx.x, xcd = bid & 7, rk = bid >> 3;
  const int p = 4 * (xcd >> 1) + (rk >> 3), j = (xcd & 1) * 8 + (rk & 7);
  const int rot = 2 * p + j;
  const unsigned lds0 = (unsigned)(size_t)L;
  LAS char* const Ll = L + lane * 16;
  const int th = j >> 3, c4 = j & 7;
  const int pos1 = w < (j & 1) ? 6 : w - (j & 1);

  unsigned vW = (lane >> 2) * 3072 + (pp ^ ((lane >> 5) << 1)) * 16;
  unsigned vX0 = (lane >> 3) * 1024 + ((lane & 7) ^ g) * 16, vX1 = (vX0 ^ 64) + 8192;
  unsigned sX = (p * 128 + w * 16) * 1024;
  unsigned sW = (w & 3) * (16 * 3072) + (1 + (w >> 2)) * 1024 + j * 64, lW = lds0 + L_WK + (w >> 2) * 32768 + (w & 3) * 1024;
#define CH(c) (((c) + rot) & 7)
  const int cres = (c4 - rot) & 7;
#define XSLOT(c) ((c) % 3 == 0 ? L_KVRED + w * 2048 : L_WO + w * 4096 + ((c) % 3 - 1) * 2048)
#define WLOAD(c) dma16(dQKV, vW, sW + CH(c) * (64 * 3072), lW + (c) * 4096)
#define XLOAD1(c, h) dma16(dX, (h) ? vX1 : vX0, sX + CH(c) * 128, lds0 + XSLOT(c) + (h) * 1024)
#define QLOAD1(n) dma16(dQKV, vW, (CH(w) * 64 + (n) * 16) * 3072 + j * 64, lds0 + L_WQ + w * 4096 + (n) * 1024)
  unsigned to = WS_EPOCH;
  asm("v_writelane_b32 %0, %1, 1\n\tv_writelane_b32 %0, %2, 2\n\tv_writelane_b32 %0, %3, 3\n\tv_writelane_b32 %0, %4, 4" : "+v"(to)
      : "s"(WS_XS + (p * 16 + j) * 128), "s"(WS_PC + ((j * 7 + pos1) * 16 + p) * 64), "s"(WS_S + ((p * 8 + 4 * th + (w >> 1)) * 16 + 2 * c4 + (w & 1)) * 128), "s"(WS_FLAG + ((w & 3) * 256 + p * 16 + j) * 128));
  asm volatile("s_waitcnt lgkmcnt(0)" : "+s"(ka), "+v"(vW), "+v"(vX0), "+v"(vX1), "+s"(sX), "+s"(sW), "+s"(lW), "+v"(to) :: "memory");
  const u16* const x = (const u16*)ka[0]; const u16* const wqkv = (const u16*)ka[1]; const u16* const wo = (const u16*)ka[2]; const u16* const w1 = (const u16*)ka[3];
  const u16* const b1 = (const u16*)ka[4]; const u16* const w2 = (const u16*)ka[5]; u16* const out = (u16*)ka[6]; char* const ws = (char*)ka[7];
  const v4u dQKV = mksrd(wqkv, 512 * 1536 * 2), dX = mksrd(x, 2048 * 512 * 2), dWS = mksrd(ws, WS_BYTES);
  const __amdgpu_buffer_rsrc_t rWS = __builtin_amdgcn_make_buffer_rsrc(ws, 0, WS_BYTES, 0x00020000);
  WLOAD(0); XLOAD1(0, 0); XLOAD1(0, 1);
  unsigned tag, bias;
  v4u xrow;
  asm volatile("buffer_load_dwordx4 %0, %1, %2, %3 offen" : "=v"(xrow) : "v"(lane * 16), "s"(dX), "s"((p * 128 + j * 8 + w) * 1024) : "memory");
  asm volatile("buffer_load_dword %0, %1, %2, 0 offen sc1" : "=v"(tag) : "v"(to), "s"(dWS) : "memory");
  if (w == 7) asm volatile("buffer_load_dword %0, %1, %2, %3 offen" : "+v"(tch) : "v"(((lane >> 3) & 1) * 3072 + (lane & 7) * 128), "s"(dQKV), "s"(bid * 6144) : "memory");
  SBAR();
  WLOAD(1); XLOAD1(1, 0); XLOAD1(1, 1);
  auto rawload = [&]() TGT {
    const unsigned c = w * 64 + lane, r = (c * 4682u) >> 16;
    if (w < 7) dma16(mksrd(w1, 512 * 1792 * 2), r * 3584 + (c - r * 14) * 16, p * (32 * 3584) + 224 * j, lds0 + L_RAW + w * 1024);
    if (w >= 6) dma16(mksrd(wo, 512 * 512 * 2), (lane >> 2) * 1024 + pp * 16, (p * 32 + (w & 1) * 16) * 1024 + c4 * 128 + th * 64, lds0 + L_RAW + 7168 + (w & 1) * 1024);
    asm volatile("global_load_dword %0, %1, %2" : "+v"(tch) : "v"((lane < 7 ? lane : 0) * 128), "s"((const char*)w2 + (bid * 8 + w) * 896) : "memory");
  };
  GAS unsigned* const flags = (GAS unsigned*)(ws + WS_FLAG);
  GAS unsigned* const myflag = flags + (p * 16 + j) * 32;
  const int hd = w & 1, tile2 = 4 * th + (w >> 1);
  LAS char* const tN = L + (8 * g + q) * 64 + pp * 8 + ((g ^ hd) & 1) * 32;
  LAS char* const kN = opq(tN + L_WK);
  LAS char* const kC = opq(tN + L_WK + ((w >> 1) & 1) * 32768 + (w >> 2) * 2048);
#define WFRAG(s, m) trfrag(kN + (m) * 32768 + (s) * 2048, 256)
  v4u ones = {ONE2, ONE2, ONE2, ONE2};
  asm volatile("" : "+v"(ones));
  v4u xf[2][16], xres;
  const f4 z4 = {0, 0, 0, 0};
  f4 ak[2] = {z4, z4}, av[2] = {z4, z4}, cs = z4;
  const int xrd = i * 128 + (g ^ (i >> 1)) * 16;
  LAS char* xp[2][2][2];
#pragma unroll
  for (int o = 0; o < 2; ++o)
#pragma unroll
    for (int h = 0; h < 2; ++h) { xp[o][0][h] = L + L_KVRED + (w ^ o) * 2048 + (xrd ^ (h * 64)); xp[o][1][h] = L + L_WO + (w ^ o) * 4096 + (xrd ^ (h * 64)); }
#define XRD(o, c, h) xf[o][2 * (c) + (h)] = ldf((c) % 3 ? xp[o][1][h] + ((c) % 3 - 1) * 2048 : xp[o][0][h])
  v4u wf[2][4], cf, xs, pr1, pro;
  f4 prc;
  f4 mr[2], rs[2];
  f2 c2k, c2v;
#define P1L(N, slot, n) dmac<N>(dWS, lane * 16, WS_P1 + (j * 112 + (slot) * 7 + (n)) * 1024, lds0 + L_W1 + ((slot) * 7 + (n)) * 1024)
#pragma unroll
  for (int c = -1; c < 8; ++c) {
    if (c >= 0) {
      ak[0] = mfma32(xf[0][2 * c], wf[c & 1][0], ak[0]);
      ak[1] = mfma32(xf[1][2 * c], wf[c & 1][0], ak[1]);
      SB();
    }
    if (c == -1) asm volatile("s_waitcnt vmcnt(3)" : "+v"(xrow), "+v"(tag), "+v"(tch) :: "memory");
    else if (c < 6) VMWAIT(c < 2 ? 7 : 3);
    else if (c == 6) {
      asm volatile("s_waitcnt vmcnt(2)" : "+v"(xs) :: "memory");
#pragma nounroll
      for (unsigned sp = 0; sp < SPIN && !none(xs.y != tag || xs.w != tag); ++sp) {
        LD16(xs, dWS, i * 16, WS_XS + (p * 128 + w * 16) * 16);
        asm volatile("s_waitcnt vmcnt(0)" : "+v"(xs) :: "memory");
      }
      if (g == 0) { *(LAS unsigned*)(L + L_STT + w * 128 + i * 4) = xs.x; *(LAS unsigned*)(L + L_STT + w * 128 + 64 + i * 4) = xs.z; }
    } else LGKM0();
    SBAR();
    if (c >= 0) {
      av[0] = mfma32(xf[0][2 * c], wf[c & 1][1], av[0]);
      av[1] = mfma32(xf[1][2 * c], wf[c & 1][1], av[1]);
    }
    if (c < 7) {
      XRD(0, c + 1, 0); XRD(1, c + 1, 0); XRD(0, c + 1, 1); XRD(1, c + 1, 1);
      cf = trfrag(kC + (c + 1) * 4096, 256);
    } else {
#pragma unroll
      for (int o = 0; o < 2; ++o) { mr[o] = *(LAS f4*)(L + L_STT + (w ^ o) * 128 + g * 16); rs[o] = *(LAS f4*)(L + L_STT + (w ^ o) * 128 + 64 + g * 16); }
      c2k = *(LAS f2*)(L + L_CKV + (hd * 16 + i) * 8); c2v = *(LAS f2*)(L + L_CKV + (32 + hd * 16 + i) * 8);
    }
    if (c < 5) WLOAD(c + 3);
    if (c == 5) LD16(xs, dWS, i * 16, WS_XS + (p * 128 + w * 16) * 16);
    SB();
    if (c >= 0) {
      ak[0] = mfma32(xf[0][2 * c + 1], wf[c & 1][2], ak[0]);
      ak[1] = mfma32(xf[1][2 * c + 1], wf[c & 1][2], ak[1]);
    }
    if (c < 7) { wf[(c + 1) & 1][0] = WFRAG(2 * c + 2, 0); wf[(c + 1) & 1][1] = WFRAG(2 * c + 2, 1); }
    if (c < 5) XLOAD1(c + 3, 0); else if (c < 7) QLOAD1(2 * c - 10);
    SB();
    if (c >= 0) {
      av[0] = mfma32(xf[0][2 * c + 1], wf[c & 1][3], av[0]);
      av[1] = mfma32(xf[1][2 * c + 1], wf[c & 1][3], av[1]);
    }
    if (c < 7) { wf[(c + 1) & 1][2] = WFRAG(2 * c + 3, 0); wf[(c + 1) & 1][3] = WFRAG(2 * c + 3, 1); }
    if (c < 5) XLOAD1(c + 3, 1); else if (c < 7) QLOAD1(2 * c - 9);
    if (c == 7) {
      st16(pr1, rWS, lane * 16, w < 7 ? WS_P1 + ((j * 16 + p) * 7 + pos1) * 1024 : WS_BYTES);
      if (i == 0) st16(__builtin_bit_cast(v4u, prc), rWS, g * 16, w < 7 ? WS_PC + ((j * 7 + pos1) * 16 + p) * 64 : WS_BYTES);
      if (w >= 6) st16(pro, rWS, lane * 16, WS_PO + ((c4 * 16 + p) * 4 + 2 * th + (w & 1)) * 1024);
    }
    SB();
    if (c < 7) {
      cs = mfma32(ones, cf, cs);
      if (c + 1 == cres) { xres = ldf(L + ((c + 1) % 3 == 0 ? L_KVRED + tile2 * 2048 : L_WO + tile2 * 4096 + ((c + 1) % 3 - 1) * 2048) + (xrd ^ (hd * 64))); LGKM0(); }
    }
    if (c == -1) {
      rawload();
      asm volatile("buffer_load_ushort %0, %1, %2, 0 offen" : "=v"(bias) : "v"((((w + 6 + (j & 1)) % 7) * 16 + pp * 4 + q) * 2), "s"(mksrd(b1 + 112 * j, 224)) : "memory");
      tag = __builtin_amdgcn_readfirstlane(tag) + 1u;
      {
        float s = dot2(xrow.x, ONE2, dot2(xrow.y, ONE2, 0.f)) + dot2(xrow.z, ONE2, dot2(xrow.w, ONE2, 0.f));
        float ss = dot2(xrow.x, xrow.x, dot2(xrow.y, xrow.y, 0.f)) + dot2(xrow.z, xrow.z, dot2(xrow.w, xrow.w, 0.f));
        s = rowsum4(dppadd<0x128>(dppadd<0x124>(dppadd<0x122>(dppadd<0x121>(s))))) * (1.f / 512.f);
        ss = rowsum4(dppadd<0x128>(dppadd<0x124>(dppadd<0x122>(dppadd<0x121>(ss))))) * (1.f / 512.f);
        const float r = __builtin_amdgcn_rcpf(rb(ln_sigma(ss - s * s)));
        if (lane == 0) st16(v4u{asu(rb(s)), tag, asu(r), tag}, rWS, 0, WS_XS + (p * 128 + j * 8 + w) * 16);
      }
    }
    if (c == 2) {
      const int il = (pp * 2 + (w & 1)) * 8;
      pr1 = trfrag(L + L_RAW + (8 * g + q) * 224 + pp * 8 + w * 32, 4 * 224);
      pro = trfrag(L + L_RAW + 7168 + (8 * g + q) * 64 + il, 256);
      asm volatile("" :: "v"(tch));
    }
    if (c == 3) prc = mfma32(pr1, ones, z4);
    if (c == 6 && g == 0) *(LAS float*)(L + L_CKV + ((w & 3) * 16 + i) * 8 + (w >> 2) * 4) = cs[0];
  }
  v4u w2r[28];
  const __amdgpu_buffer_rsrc_t rW2 = __builtin_amdgcn_make_buffer_rsrc((void*)w2, 0, 1792 * 512 * 2, 0x00020000);
  const int vW2 = (((lane >> 3) & 1) * 16 + g * 4) * 1024 + (lane & 7) * 16, vW2n = (((lane >> 3) & 1) * 4 + g * 8) * 1024 + (lane & 7) * 16, sW2 = 224 * w * 1024 + c4 * 128;
#define W2L(n) w2r[n] = __builtin_amdgcn_raw_buffer_load_b128(rW2, (((n) >> 2) == 3 ? vW2n : vW2) + ((n) & 3) * 1024, sW2 + ((n) >> 2) * 32768, 0)
  {
    const float ck = c2k[0] + c2k[1], cv = c2v[0] + c2v[1];
    float kk[8], vv[8];
#pragma unroll
    for (int n = 0; n < 8; ++n) {
      kk[n] = phi((ak[n >> 2][n & 3] - mr[n >> 2][n & 3] * ck) * rs[n >> 2][n & 3]);
      vv[n] = (av[n >> 2][n & 3] - mr[n >> 2][n & 3] * cv) * rs[n >> 2][n & 3];
    }
    const v4u pkf = {pk(kk[0], kk[1]), pk(kk[2], kk[3]), pk(kk[4], kk[5]), pk(kk[6], kk[7])};
    const v4u pvf = {pk(vv[0], vv[1]), pk(vv[2], vv[3]), pk(vv[4], vv[5]), pk(vv[6], vv[7])};
    const f4 kv = mfma32(pkf, pvf, z4), z = mfma32(pkf, ones, z4);
    *(LAS f4*)(Ll + L_KVRED + w * 1024) = kv;
    if (i == 0) *(LAS f4*)(L + L_ZP + w * 64 + g * 16) = z;
  }
  if (w < 6) VMWAIT(2); else VMWAIT(3);
  BAR();
  if (w < 2) {
    __builtin_amdgcn_s_setprio(3);
    f4 t[8];
#pragma unroll
    for (int n = 0; n < 4; ++n) { t[n] = *(LAS f4*)(Ll + L_KVRED + (n * 2 + w) * 1024); t[4 + n] = *(LAS f4*)(L + L_ZP + (n * 2 + w) * 64 + pp * 16); }
    SB();
    const int so = WS_KV + ((j * 2 + w) * 16 + p) * KV_REC;
    const f4 a = (t[0] + t[1]) + (t[2] + t[3]), z = (t[4] + t[5]) + (t[6] + t[7]);
    st16(v4u{pk(a[0], a[1]), tag, pk(a[2], a[3]), tag}, rWS, lane * 16, so);
    if (lane < 8) st16(v4u{pk(z[0], z[1]), tag, pk(z[2], z[3]), tag}, rWS, lane * 16 + 1024, so);
  }
  v2u pq[2];
  v4u kt[5];
  const unsigned kso = WS_KV + ((j * 2 + hd) * 16 + 4 * (w >> 1)) * KV_REC;
  {
    f4 aq[2] = {z4, z4}, cq = z4;
    v4u qf[4];
    float m1[2], r1[2];
    f4 cp[4];
#pragma unroll
    for (int s = 0; s < 3; ++s) qf[s] = trfrag(tN + L_WQ + s * 2048, 256);
    {
      v4u t[4];
#pragma unroll
      for (int n = 0; n < 4; ++n) t[n] = trfrag(tN + L_WQ + (w >> 1) * 8192 + n * 2048, 256);
#pragma unroll
      for (int n = 0; n < 4; ++n) cq = mfma32(t[n], ones, cq);
    }
#pragma unroll
    for (int s = 0; s < 16; ++s) {
      if (s + 3 < 16) qf[(s + 3) & 3] = trfrag(tN + L_WQ + (s + 3) * 2048, 256);
      aq[0] = mfma32(qf[s & 3], xf[0][s], aq[0]);
      aq[1] = mfma32(qf[s & 3], xf[1][s], aq[1]);
      if (s == 1 && i == 0) *(LAS f4*)(L + L_CQ + w * 64 + g * 16) = cq;
      if (s >= 2 && s < 2 + W2Q) W2L(s - 2);
      if (s == 13) {
#pragma unroll
        for (int o = 0; o < 2; ++o) { m1[o] = *(LAS float*)(L + L_STT + (w ^ o) * 128 + i * 4); r1[o] = *(LAS float*)(L + L_STT + (w ^ o) * 128 + 64 + i * 4); }
      }
      if (s == 14) {
        VMWAIT(W2Q);
        __builtin_amdgcn_s_setprio(0);
        SBAR();
#pragma unroll
        for (int n = 0; n < 4; ++n) cp[n] = *(LAS f4*)(L + L_CQ + (2 * n + hd) * 64 + g * 16);
      }
      if (s == 15) {
#pragma unroll
        for (int n = 0; n < 4; ++n) LD16(kt[n], dWS, lane * 16, kso + n * KV_REC);
        LD16(kt[4], dWS, q * KV_REC + pp * 16 + 1024, kso);
      }
      SB();
    }
#pragma unroll
    for (int n = W2Q; n < W2Q + W2R; ++n) W2L(n);
    SB();
    if (tid == 128) __hip_atomic_store(myflag, tag, RLX_AGENT);
    cq = (cp[0] + cp[1]) + (cp[2] + cp[3]);
#pragma unroll
    for (int o = 0; o < 2; ++o) {
      const float m = m1[o], r = r1[o];
      float a[4];
#pragma unroll
      for (int n = 0; n < 4; ++n) a[n] = phi((aq[o][n] - m * cq[n]) * r);
      pq[o] = v2u{pk(a[0], a[1]), pk(a[2], a[3])};
    }
    asm volatile("" : "+v"(pq[0]), "+v"(pq[1]));
  }
  LAS char* const cw = opq(Ll + L_CONS + w * 1024);
  LAS char* const zw = opq(L + L_ZC + w * 64 + pp * 16);
  LAS char* const cr = opq(Ll + L_CONS + hd * 1024);
  LAS char* const zr = opq(L + L_ZC + hd * 64 + g * 16);
  unsigned vo = ((hd ^ g) & 1) * 16384 + ((2 * hd + (g >> 1)) * 16 + i) * 16, vf = (i * 16 + (j ^ ((g & 1) << 3))) * 128;
  asm volatile("" : "+v"(vo), "+v"(vf));
  {
    asm volatile("s_waitcnt vmcnt(%5)" : "+v"(kt[0]), "+v"(kt[1]), "+v"(kt[2]), "+v"(kt[3]), "+v"(kt[4]) : "i"(W2R) : "memory");
#pragma nounroll
    for (unsigned sp = 0; sp < SPIN; ++sp) {
      unsigned lo = kt[0].y & kt[0].w, hi = kt[0].y | kt[0].w;
#pragma unroll
      for (int n = 1; n < 5; ++n) { lo &= kt[n].y & kt[n].w; hi |= kt[n].y | kt[n].w; }
      asm volatile("" : "+v"(lo), "+v"(hi));
      if (none(lo != tag || hi != tag)) break;
#pragma unroll
      for (int n = 0; n < 4; ++n) LD16(kt[n], dWS, lane * 16, kso + n * KV_REC);
      LD16(kt[4], dWS, q * KV_REC + pp * 16 + 1024, kso);
      asm volatile("s_waitcnt vmcnt(0)" : "+v"(kt[0]), "+v"(kt[1]), "+v"(kt[2]), "+v"(kt[3]), "+v"(kt[4]) :: "memory");
    }
    f4 s = {bflo(kt[0].x), bfhi(kt[0].x), bflo(kt[0].z), bfhi(kt[0].z)};
#pragma unroll
    for (int n = 1; n < 4; ++n) s += f4{bflo(kt[n].x), bfhi(kt[n].x), bflo(kt[n].z), bfhi(kt[n].z)};
    *(LAS f4*)cw = s;
    const f4 z = {dppadd<0x124>(dppadd<0x128>(bflo(kt[4].x))), dppadd<0x124>(dppadd<0x128>(bfhi(kt[4].x))),
                  dppadd<0x124>(dppadd<0x128>(bflo(kt[4].z))), dppadd<0x124>(dppadd<0x128>(bfhi(kt[4].z)))};
    *(LAS f4*)zw = z;
  }
#pragma unroll
  for (int n = W2Q + W2R; n < W2Q + W2R + W2S; ++n) W2L(n);
  BAR();
  unsigned pf = tag;
  if (w == 0) asm volatile("buffer_load_dword %0, %1, %2, %3 offen sc1" : "+v"(pf) : "v"(vf), "s"(dWS), "s"(WS_FLAG) : "memory");
  const int ohoff = ((p * 8 + w) * 16 + j) * 1024;
  {
    f4 kq[4], zq[4];
#pragma unroll
    for (int n = 0; n < 4; ++n) { zq[n] = *(LAS f4*)(zr + n * 128); kq[n] = *(LAS f4*)(cr + n * 2048); }
    SB();
    const f4 k0 = (kq[0] + kq[1]) + (kq[2] + kq[3]), z0 = (zq[0] + zq[1]) + (zq[2] + zq[3]);
    const v2u ka = {pk(k0[0], k0[1]), pk(k0[2], k0[3])}, za = {pk(z0[0], z0[1]), pk(z0[2], z0[3])};
    const f4 n0 = mfma16(ka, pq[0], z4), n1 = mfma16(ka, pq[1], z4), d0 = mfma16(za, pq[0], z4), d1 = mfma16(za, pq[1], z4);
    const float r0 = __builtin_amdgcn_rcpf(d0[0] + 1e-6f), r1 = __builtin_amdgcn_rcpf(d1[0] + 1e-6f);
    const auto sx = __builtin_amdgcn_permlane16_swap(pk(n0[0] * r0, n0[1] * r0), pk(n1[0] * r1, n1[1] * r1), false, false);
    const auto sy = __builtin_amdgcn_permlane16_swap(pk(n0[2] * r0, n0[3] * r0), pk(n1[2] * r1, n1[3] * r1), false, false);
    st16(v4u{sx[0], sy[0], sx[1], sy[1]}, rWS, vo, WS_O + ohoff - hd * 16384);
  }
  constexpr int W2E = W2Q + W2R + W2S + W2T;
#pragma unroll
  for (int n = W2E - W2T; n < W2E; ++n) W2L(n);
  asm volatile("s_waitcnt vmcnt(%1)" : "+v"(pf) : "i"(W2T) : "memory");
  if (!none(pf != tag)) poll(flags + (i * 16 + (j ^ ((g & 1) << 3))) * 32, tag, 32, lane);
  SBAR();
  if (tid == 0) __hip_atomic_store(myflag + 256 * 32, tag, RLX_AGENT);
  {
    const unsigned so = WS_PO + (c4 * 16 + 2 * w) * 4096;
    dmac<4>(dWS, lane * 16, so, lds0 + L_WO2 + w * 8192);
    dmac<4>(dWS, lane * 16, so + 4096, lds0 + L_WO2 + w * 8192 + 4096);
  }
  v4u ct;
  LD16(ct, dWS, lane * 16, WS_PC + (j * 7 + w - 1) * 1024);
  w2pack(w2r); SB();
  w2pack(w2r + 4); SB();
  if (w == 0) poll(flags + (256 + p * 16 + i) * 32, tag, 16, lane);
  asm volatile("s_waitcnt vmcnt(0)" : "+v"(ct), "+v"(bias) :: "memory");
  __builtin_amdgcn_s_waitcnt(0x0F70);
  SBAR();
#define LOAD1(s) f[s] = __builtin_amdgcn_raw_buffer_load_b128(rWS, lane * 16 + ((s) & 3) * 1024, so + ((s) & 12) * 1024, SC1)
#define LOADPD() do { _Pragma("unroll") for (int s = 0; s < PD; ++s) { LOAD1(s); SB(); } } while (0)
  v4u hres;
  {
    const unsigned so = WS_O + (p * 8 + tile2) * 16384 + hd * 8192;
    v4u f[8];
    LAS char* const Lo = opq(Ll + L_WO2 + hd * (32768 + 2048));
    LAS char* const Lp = opq(Ll + L_WO2 + hd * (32768 - 2048) + 2048);
    SFOR(s, 0, 8, 1)
      ld16i<(s & 3) * 1024>(f[s], dWS, lane * 16, so + (s & 4) * 1024);
    SEND
    v4u wf2[2][4];
#pragma unroll
    for (int t = 0; t < 4; ++t) wf2[0][t] = ldf((t < 2 ? Lo : Lp) + (t & 1) * 1024);
    f4 a[4] = {f4{bflo(xres.x), bfhi(xres.x), bflo(xres.y), bfhi(xres.y)}, f4{bflo(xres.z), bfhi(xres.z), bflo(xres.w), bfhi(xres.w)}, z4, z4};
    SFOR(s, 0, 8, 1)
      if (s + 1 < 8) {
#pragma unroll
        for (int t = 0; t < 4; ++t) wf2[(s + 1) & 1][t] = ldf((t < 2 ? Lo : Lp) + (s + 1) * 4096 + (t & 1) * 1024);
      }
      vmpin<7 - s + (s < P2T ? s : P2T)>(f[s]);
      SB();
#pragma unroll
      for (int t = 3; t >= 0; --t) a[t] = mfma32(wf2[s & 1][t], f[s], a[t]);
      if (s < P2T) P1L(1, w, s);
      SB();
    SEND
    *(LAS f4*)(Ll + L_EX + w * 2048) = a[2];
    *(LAS f4*)(Ll + L_EX + w * 2048 + 1024) = a[3];
    BAR();
    const f4 a0 = a[0] + *(LAS f4*)(Ll + L_EX + (w ^ 1) * 2048), a1 = a[1] + *(LAS f4*)(Ll + L_EX + (w ^ 1) * 2048 + 1024);
    hres = v4u{pk(a0[0], a0[1]), pk(a0[2], a0[3]), pk(a1[0], a1[1]), pk(a1[2], a1[3])};
    const int ho = ((p * 8 + tile2) * 16 + 2 * c4 + hd) * 1024;
    st16(hres, rWS, lane * 16, WS_H + ho);
    const f4 hs = mfma32(ones, hres, z4), hg = mfma32(hres, hres, z4);
    if (g == q) __builtin_amdgcn_raw_buffer_store_b64(v2u{asu(hs[0]), asu(pp == 0 ? hg[0] : pp == 1 ? hg[1] : pp == 2 ? hg[2] : hg[3])}, rWS, i * 16, WS_S + (ho >> 3) - hd * 120, SC1);
    if (bid == 0 && tid == 0) __builtin_amdgcn_raw_buffer_store_b32(tag, rWS, WS_EPOCH, 0, SC1);
  }
  SFOR(n, P2T, 7, 1) P1L(1, w, n); SEND
    if (w) {
      const f4 c = {rowsum4(dppadd<0x128>(dppadd<0x124>(asf(ct.x)))), rowsum4(dppadd<0x128>(dppadd<0x124>(asf(ct.y)))),
                    rowsum4(dppadd<0x128>(dppadd<0x124>(asf(ct.z)))), rowsum4(dppadd<0x128>(dppadd<0x124>(asf(ct.w))))};
      const float cf = q == 0 ? c[0] : q == 1 ? c[1] : q == 2 ? c[2] : c[3];
      const unsigned ch = pk(cf, cf), cl = pk(cf - bflo(ch), 0.f);
      if (g == 0) *(LAS v4u*)(L + L_C1 + ((w - 1) * 16 + pp * 4 + q) * 16) = v4u{(ch & 0xffffu) | (bias << 16), cl, 0u, 0u};
    }
  VMWAIT(7 - P2T);
  BAR();
  if (tid == 0) __hip_atomic_store(myflag + 512 * 32, tag, RLX_AGENT);
  P1L(4, 8 + w, 0); P1L(3, 8 + w, 4);
#pragma unroll
  for (int s = 2; s < W2E / 4; ++s) { w2pack(w2r + 4 * s); SB(); }
  v4u cb[7];
#pragma unroll
  for (int t = 0; t < 7; ++t) cb[t] = ldf(L + L_C1 + (t * 16 + i) * 16);
  if (w == 0) poll(flags + (512 + p * 16 + i) * 32, tag, 16, lane);
  VMWAIT(0);
  SBAR();
  constexpr int W2B = W2E + W2P;
  {
    v4u f[16];
    const int so = WS_H + (p * 8 + w) * 16384;
    LAS char* const wp0 = opq(Ll + L_W1);
    LAS char* const wp1 = opq(wp0 + 8 * 7168);
#define TS(n) ((n) < 16 * NA ? (n) / NA : ((n) - 16 * NA) / (7 - NA))
#define TT(n) ((n) < 16 * NA ? (n) % NA : NA + ((n) - 16 * NA) % (7 - NA))
#define W1F(n) ldf((TS(n) < 8 ? wp0 : wp1) + (TS(n) & 7) * 7168 + TT(n) * 1024)
    v2u e[7];
    const int aoff = WS_A + ((p * 8 + w) * 56 + ((7 * j + 1) >> 1)) * 1024;
    f4 acc[7];
    __builtin_amdgcn_s_setprio(2);
    v4u st[2];
#pragma unroll
    for (int n = 0; n < 2; ++n) st[n] = __builtin_amdgcn_raw_buffer_load_b128(rWS, g * 512 + i * 16 + n * 256, WS_S + (p * 8 + w) * 2048, SC1);
    SB();
    LOADPD();
    float rstd2;
    unsigned msk = g ? 0u : ~0u;
    asm volatile("" : "+v"(msk));
    {
      v4u wf3[6];
#pragma unroll
      for (int n = 0; n < 4; ++n) wf3[n] = W1F(n);
      SB();
      {
        const float mean2 = rowsum4((asf(st[0].x) + asf(st[0].z)) + (asf(st[1].x) + asf(st[1].z))) * (1.f / 512.f);
        const float sig = ln_sigma(rowsum4((asf(st[0].y) + asf(st[0].w)) + (asf(st[1].y) + asf(st[1].w))) * (1.f / 512.f) - mean2 * mean2);
        const unsigned ms = pk(-mean2, sig);
        rstd2 = __builtin_amdgcn_rcpf(bfhi(ms));
        const v4u ex = {ms & msk, ms & msk, 0u, 0u};
#pragma unroll
        for (int t = 0; t < 7; ++t) acc[t] = mfma32(cb[t], ex, z4);
      }
      auto epi = [&](int t0, int t1) __attribute__((always_inline)) TGT {
        __builtin_amdgcn_s_setprio(0);
#pragma unroll
        for (int t = t0; t < t1; ++t) {
          const f4 a = acc[t] * (rstd2 * LOG2E);
          const f4 m = f4{__builtin_amdgcn_exp2f(a[0]), __builtin_amdgcn_exp2f(a[1]), __builtin_amdgcn_exp2f(a[2]), __builtin_amdgcn_exp2f(a[3])} + 1.f;
          const f4 d = m * m + 1.f;
          const f4 y = acc[t] * (f4{__builtin_amdgcn_rcpf(d[0]), __builtin_amdgcn_rcpf(d[1]), __builtin_amdgcn_rcpf(d[2]), __builtin_amdgcn_rcpf(d[3])} * (-2.f * rstd2) + rstd2);
          e[t] = v2u{pk(y[0], y[1]), pk(y[2], y[3])};
          if (t & 1) { st16(v4u{e[t - 1].x, e[t - 1].y, e[t].x, e[t].y}, rWS, lane * 16 + (t >> 1) * 1024, aoff); SB(); }
        }
        SB();
      };
      SFOR(n, 0, 112, 2)
        if (n == 16 * NA) { epi(0, NA); __builtin_amdgcn_s_setprio(2); }
        if (n + 4 < 112) { wf3[(n + 4) % 6] = W1F(n + 4); wf3[(n + 5) % 6] = W1F(n + 5); }
        SB();
        acc[TT(n + 1)] = mfma32(wf3[(n + 1) % 6], f[TS(n + 1)], acc[TT(n + 1)]);
        acc[TT(n)] = mfma32(wf3[n % 6], f[TS(n)], acc[TT(n)]);
        constexpr int k = n / 4 + PD;
        if ((n & 2) == 0) {
          if (k < 16) LOAD1(k < 16 ? k : 0);
          else if (W2E + k - 16 < W2B) W2L(k > 15 && W2E + k - 16 < W2B ? W2E + k - 16 : 0);
        }
        SB();
      SEND
      epi(NA, 7);
    }
#define SINGLE(v, off) do { const auto a_ = __builtin_amdgcn_permlane16_swap(v.x, v.x, false, false), b_ = __builtin_amdgcn_permlane16_swap(v.y, v.y, false, false); \
      if ((g & 1) == 0) st16(v4u{a_[0], b_[0], a_[1], b_[1]}, rWS, (g >> 1) * 256 + i * 16, aoff + (off)); } while (0)
    SINGLE(e[6], (j & 1) ? -512 : 3072);
  }
#pragma unroll
  for (int n = W2B; n < W2B + W2X; ++n) W2L(n);
  VMWAIT(W2X);
  BAR();
  if (tid == 0) __hip_atomic_store(myflag + 768 * 32, tag, RLX_AGENT);
#pragma unroll
  for (int n = W2B + W2X; n < 28; ++n) W2L(n);
  SB();
  *(LAS v4u*)(L + L_HRES + w * 1024 + (16 * g + (i ^ (4 * g))) * 16) = hres;
#pragma unroll
  for (int s = W2E / 4; s < (W2B + W2X) / 4; ++s) { w2pack(w2r + 4 * s); SB(); }
  poll(flags + (768 + p * 16 + 2 * w + (lane & 1)) * 32, tag, 2, lane);
  {
    const int so = WS_A + ((p * 8 + 4 * th) * 56 + 7 * w) * 1024;
    v4u f[NB];
#define ALOAD(n) __builtin_amdgcn_raw_buffer_load_b128(rWS, lane * 16 + (((n) % 7) & 3) * 1024, so + (((n) / 7) * 56 + (((n) % 7) & 4)) * 1024, SC1)
#pragma unroll
    for (int n = 0; n < NB; ++n) { f[n] = ALOAD(n); SB(); }
#pragma unroll
    for (int s = (W2B + W2X) / 4; s < 7; ++s) { w2pack(w2r + 4 * s); SB(); }
    const int wr = (w * 1024 + 16 * g + (i ^ ((g & 1) << 3))) * 16;
    f4 acc[2][4];
#define PUT(t, r) *(LAS f4*)(L + (wr ^ ((r) << 5)) + (t) * 4096 + (r) * 1024) = f4{acc[(t) & 1][0][r], acc[(t) & 1][1][r], acc[(t) & 1][2][r], acc[(t) & 1][3][r]}
#pragma unroll
    for (int n = 0; n < 28; ++n) {
      const int s = n % 7, t = n / 7;
#pragma unroll
      for (int u = 0; u < 4; ++u) acc[t & 1][u] = mfma32(w2r[s * 4 + u], f[n % NB], s ? acc[t & 1][u] : z4);
      if (n + NB < 28) f[n % NB] = ALOAD(n + NB < 28 ? n + NB : 0);
      if (t && s >= 1 && s < 5) PUT(t - 1, (s - 1) & 3);
      if (n == 27) {
#pragma unroll
        for (int r = 0; r < 4; ++r) PUT(t, r);
      }
      SB();
    }
    BAR();
    const int tk = hd * 8 + (lane >> 3);
    LAS char* const rp = opq(L + (w >> 1) * 4096 + pp * 1024 + (16 * (q & 1) + (tk ^ ((q & 1) << 3) ^ (pp << 1))) * 16);
    LAS char* const rq = opq(rp + 65536);
    hres = *(LAS v4u*)(L + L_HRES + ((w & 6) + (q & 1)) * 1024 + (16 * pp + (tk ^ (4 * pp))) * 16);
    f4 t[16];
#pragma unroll
    for (int n = 0; n < 16; ++n) t[n] = *(LAS f4*)((n & 4 ? rq : rp) + (n & 3) * 16384 + (n >> 3) * 512);
    SB();
    const f4 a0 = ((t[0] + t[1]) + (t[2] + t[3])) + ((t[4] + t[5]) + (t[6] + t[7]));
    const f4 a1 = ((t[8] + t[9]) + (t[10] + t[11])) + ((t[12] + t[13]) + (t[14] + t[15]));
    const v4u o = {pk(a0[0] + bflo(hres.x), a0[1] + bfhi(hres.x)), pk(a0[2] + bflo(hres.y), a0[3] + bfhi(hres.y)),
                   pk(a1[0] + bflo(hres.z), a1[1] + bfhi(hres.z)), pk(a1[2] + bflo(hres.w), a1[3] + bfhi(hres.w))};
    __builtin_amdgcn_raw_buffer_store_b128(o, __builtin_amdgcn_make_buffer_rsrc(out, 0, 2048 * 512 * 2, 0x00020000), ((p * 128 + th * 64 + w * 8 + (lane >> 3)) * 512 + 64 * c4 + 8 * (lane & 7)) * 2, 0, 17);
  }
}

extern "C" void kernel_launch(void* const* d_in, const int* in_sizes, int n_in, void* d_out, int out_size, void* d_ws, size_t ws_size,
                              hipStream_t stream) {
  (void)in_sizes; (void)n_in; (void)out_size; (void)ws_size;
  (void)hipFuncSetAttribute((const void*)fused_block, hipFuncAttributeMaxDynamicSharedMemorySize, LDS_BYTES);
  fused_block<<<256, 512, LDS_BYTES, stream>>>((const u16*)d_in[0], (const u16*)d_in[1], (const u16*)d_in[2], (const u16*)d_in[4],
                                               (const u16*)d_in[5], (const u16*)d_in[6], (u16*)d_out, (char*)d_ws);
}
```
